```python
import torch

def _softmax(x, axis=-1):
    return torch.softmax(x, dim=axis)
def _gelu(x):
    return torch.nn.functional.gelu(x, approximate="tanh")
def _silu(x):
    return torch.nn.functional.silu(x)
def _sigmoid(x):
    return torch.sigmoid(x)
def _layernorm(x, axis=-1, eps=1e-5):
    m = x.mean(dim=axis, keepdim=True)
    v = x.var(dim=axis, keepdim=True, unbiased=False)
    return (x - m) / torch.sqrt(v + eps)
def _scatter_add(tgt, idx, src, axis=0):
    return tgt.clone().index_add_(axis, idx, src)
def _segment_sum(src, seg, n):
    out = torch.zeros((n,) + src.shape[1:], dtype=src.dtype)
    return out.index_add_(0, seg, src)
def _pad(x, pw):
    flat = tuple(v for pair in reversed(pw) for v in pair)
    return torch.nn.functional.pad(x, flat)

kernel_name = 'interleave_layers__ssm_conv_s4d__glu_gated_glu__prompt'

def setup_inputs():

    _w = (lambda a: (lambda t: t.to(torch.bfloat16) if t.is_floating_point() else t)(torch.as_tensor(a)))
    def _A_setup():
        Bsz, L, D, N = 1, 2048, 448, 256
        return {
            "u": _w((lambda *s: torch.randn(*s, dtype=torch.bfloat16) * 0.05)(Bsz, L, D)),
            "log_A_real": _w((lambda *s: torch.randn(*s, dtype=torch.bfloat16) * 3.2)(D, N) * 0.5),
            "A_imag": _w((lambda *s: torch.randn(*s, dtype=torch.bfloat16) * 0.1)(D, N) * 3.14),
            "C_real": _w((lambda *s: torch.randn(*s, dtype=torch.bfloat16) * 0.1)(D, N) / N ** 0.5),
            "C_imag": _w((lambda *s: torch.randn(*s, dtype=torch.bfloat16) * 0.1)(D, N) / N ** 0.5),
            "log_dt": _w((lambda *s: torch.randn(*s, dtype=torch.bfloat16) * 0.1)(D) * 0.5 - 4.0),
            "D_skip": _w((lambda *s: torch.randn(*s, dtype=torch.bfloat16) * 0.1)(D)),
        }
    _A_in = _A_setup()

    def _B_setup():
        B, S, D, F = 1, 2048, 448, 1792
        return {
            "x": _w((lambda *s: torch.randn(*s, dtype=torch.bfloat16) * 0.05)(B, S, D)),
            "w_gate": _w((lambda *s: torch.randn(*s, dtype=torch.bfloat16) * 0.05)(D, F)),
            "w_up": _w((lambda *s: torch.randn(*s, dtype=torch.bfloat16) * 0.05)(D, F)),
            "w_down": _w((lambda *s: torch.randn(*s, dtype=torch.bfloat16) * 0.05)(F, D)),
        }
    _B_in = _B_setup()

    return {
        "A_u": _A_in["u"],
        "A_log_A_real": _A_in["log_A_real"],
        "A_A_imag": _A_in["A_imag"],
        "A_C_real": _A_in["C_real"],
        "A_C_imag": _A_in["C_imag"],
        "A_log_dt": _A_in["log_dt"],
        "A_D_skip": _A_in["D_skip"],
        "B_x": _B_in["x"],
        "B_w_gate": _B_in["w_gate"],
        "B_w_up": _B_in["w_up"],
        "B_w_down": _B_in["w_down"],
    }

def reference(A_u, A_log_A_real, A_A_imag, A_C_real, A_C_imag, A_log_dt, A_D_skip, B_x, B_w_gate, B_w_up, B_w_down):
    def _A_ref(u, log_A_real, A_imag, C_real, C_imag, log_dt, D_skip):
        L = u.shape[1]
        dt = torch.exp(log_dt)
        A = -torch.exp(log_A_real) + 1j * A_imag
        C = (C_real + 1j * C_imag) * dt[:, None]
        pos = (lambda x, dt: x.to(dt))(torch.arange(L), log_dt.dtype)
        P = torch.exp((A * dt[:, None])[:, :, None] * pos[None, None, :])
        K = 2.0 * torch.einsum("dn,dnl->dl", C, P).real
        L2 = 2 * L
        u_t = (lambda x, *d: x.permute(*d))(u, 0, 2, 1)
        Kf = (lambda x, *a, **k: torch.fft.rfft(x.float(), *a, **k))(K, L2)
        Uf = (lambda x, *a, **k: torch.fft.rfft(x.float(), *a, **k))(u_t, L2)
        y = (lambda x, *a, **k: torch.fft.irfft(x, *a, **k).to(torch.bfloat16))(Uf * Kf[None, :, :], L2)[:, :, :L]
        y = (lambda x, *d: x.permute(*d))(y, 0, 2, 1)
        return y + u * D_skip[None, None, :]

    def _B_ref(x, w_gate, w_up, w_down):
        g = torch.einsum("bsd,df->bsf", x, w_gate)
        u = torch.einsum("bsd,df->bsf", x, w_up)
        h = _sigmoid(g) * u
        return torch.einsum("bsf,fd->bsd", h, w_down)

    x = A_u
    x = x + _A_ref(_layernorm(x, axis=-1), A_log_A_real, A_A_imag, A_C_real, A_C_imag, A_log_dt, A_D_skip)
    x = x + _B_ref(_layernorm(x, axis=-1), B_w_gate, B_w_up, B_w_down)
    x = x + _A_ref(_layernorm(x, axis=-1), A_log_A_real, A_A_imag, A_C_real, A_C_imag, A_log_dt, A_D_skip)
    x = x + _B_ref(_layernorm(x, axis=-1), B_w_gate, B_w_up, B_w_down)
    return x
```

```cpp
#include <hip/hip_runtime.h>
#include <hip/hip_bf16.h>

namespace {

#define GAS __attribute__((address_space(1)))
#define LAS __attribute__((address_space(3)))
#define DI __device__ __forceinline__
#define GAP() __builtin_amdgcn_sched_barrier(0)
#define RLX_AGENT __ATOMIC_RELAXED, __HIP_MEMORY_SCOPE_AGENT
typedef float f32x16 __attribute__((ext_vector_type(16)));
typedef float f32x4 __attribute__((ext_vector_type(4)));
typedef float f32x2 __attribute__((ext_vector_type(2)));
typedef __bf16 bf16x2 __attribute__((ext_vector_type(2)));
typedef _Float16 f16x2 __attribute__((ext_vector_type(2)));
typedef short s16x4 __attribute__((ext_vector_type(4)));
typedef unsigned u32x4 __attribute__((ext_vector_type(4)));
typedef unsigned u32x2 __attribute__((ext_vector_type(2)));
typedef __amdgpu_buffer_rsrc_t rsrc_t;
typedef LAS unsigned char* lptr;
typedef unsigned short u16;

constexpr int L_ = 2048, D_ = 448, N_ = 256, F_ = 1792;
constexpr int SC1 = 16;
constexpr unsigned SPIN_CAP = 1u << 17;
constexpr unsigned CODE_PREFETCH = 48 * 1024;

constexpr unsigned WS_EPOCH = 0;
constexpr unsigned WS_FX = 4096;
constexpr unsigned WS_FH = WS_FX + 16384;
constexpr unsigned WS_F2 = WS_FH + 65536;
constexpr unsigned WS_ST = WS_F2 + 65536;
constexpr unsigned WS_PS = WS_ST + 65536;
constexpr unsigned WS_CS = WS_PS + 128 * 7 * 32 * 8;
constexpr unsigned WS_CTL_END = WS_CS + 2 * F_ * 8;
constexpr unsigned ACT_BYTES = L_ * D_ * 2;
constexpr unsigned WS_XT = 1u << 20;
constexpr unsigned WS_X1 = WS_XT + 2 * ACT_BYTES;
constexpr unsigned H_BYTES = L_ * F_ * 2;
constexpr unsigned WS_H = WS_X1 + 2 * ACT_BYTES;
constexpr unsigned WS_DBG = WS_H + 2 * H_BYTES;
constexpr unsigned WS_TOTAL = WS_DBG + (1u << 20);
static_assert(WS_CTL_END <= WS_XT, "control block");

constexpr unsigned T_FX = 1, T_FH = 16, T_F2 = 32, T_ST = 64, T_PS = 80, T_CS = 96;

constexpr unsigned LA_U = 128;
constexpr unsigned U_CH = 7600;
constexpr unsigned LA_KT = 15360;
constexpr unsigned KT_CP = 4160, KT_CH = 4 * KT_CP;
constexpr unsigned LA_Y = 0;
constexpr unsigned XB_SZ = 28672;
constexpr unsigned PT_W = 5120, PT_SZ = 4 * PT_W;
constexpr unsigned TB_W = 53 * 272;
constexpr unsigned LA_RED = 57856;
constexpr unsigned LA_MISC = 69952;
constexpr unsigned LA_DUMP = 70656;
constexpr unsigned LA_HS = 71680;
constexpr unsigned LA_KC = 73216;
constexpr unsigned LA_W = 81664;
constexpr unsigned W_PL = 40960;
constexpr unsigned LDS_BYTES = LA_W + 81920;
static_assert(LDS_BYTES <= 163840 && 4 * TB_W <= LA_RED && LA_RED + 4 * 21 * 36 * 4 <= LA_MISC, "lds map");

struct Params {
  const u16 *u, *lar, *aim, *cre, *cim, *ldt, *dsk, *wg, *wu, *wd;
  u16* out;
  unsigned char* ws;
};

DI rsrc_t mk_rsrc(const void* p, unsigned bytes) { return __builtin_amdgcn_make_buffer_rsrc((void*)p, 0, (int)bytes, 0x00020000); }
DI float asf(unsigned x) { return __builtin_bit_cast(float, x); }
DI float bflo(unsigned x) { return __builtin_bit_cast(float, x << 16); }
DI float bfhi(unsigned x) { return __builtin_bit_cast(float, x & 0xffff0000u); }
DI unsigned pkbf(float lo, float hi) { f32x2 v = {lo, hi}; return __builtin_bit_cast(unsigned, __builtin_convertvector(v, bf16x2)); }
DI unsigned pkbf_s(float lo, float hi) { unsigned r; asm("v_cvt_pk_bf16_f32 %0, %1, %2" : "=v"(r) : "v"(lo), "v"(hi)); return r; }
DI unsigned pkh(float lo, float hi) { f32x2 v = {lo, hi}; return __builtin_bit_cast(unsigned, __builtin_convertvector(v, f16x2)); }
DI float rbf(float x) { return bflo(pkbf(x, x) & 0xffffu); }
DI u32x2 trr(lptr p) { return __builtin_bit_cast(u32x2, __builtin_amdgcn_ds_read_tr16_b64_v4i16((LAS s16x4*)p)); }
DI u32x4 cat(u32x2 a, u32x2 b) { return u32x4{a.x, a.y, b.x, b.y}; }
DI unsigned ld_flag(unsigned char* ws, unsigned off) { return __hip_atomic_load((GAS unsigned*)(ws + off), RLX_AGENT); }
DI void st_flag(unsigned char* ws, unsigned off, unsigned v) { __hip_atomic_store((GAS unsigned*)(ws + off), v, RLX_AGENT); }
DI void st_gran(unsigned char* ws, unsigned off, unsigned val, unsigned tag) {
  __hip_atomic_store((GAS unsigned long long*)(ws + off), ((unsigned long long)tag << 32) | val, RLX_AGENT);
}
#define BAR() asm volatile("s_waitcnt lgkmcnt(0)\n\ts_barrier" ::: "memory")
#define VMWAIT(n) asm volatile("s_waitcnt vmcnt(" #n ")" ::: "memory")
#define ORDER() __builtin_amdgcn_fence(__ATOMIC_ACQUIRE, "wavefront")

template <int CTRL> DI float dpp_add(float x) {
  return x + __builtin_bit_cast(float, __builtin_amdgcn_update_dpp(0, __builtin_bit_cast(int, x), CTRL, 0xf, 0xf, true));
}
template <int CTRL> DI unsigned dpp_umax(unsigned x) {
  unsigned y = (unsigned)__builtin_amdgcn_update_dpp(0, (int)x, CTRL, 0xf, 0xf, true); return x > y ? x : y;
}
constexpr int DPP_X1 = 0xB1, DPP_X2 = 0x4E, DPP_ROR4 = 0x124, DPP_ROR8 = 0x128;
DI float sw32_add(float a, float b) {
  auto r = __builtin_amdgcn_permlane32_swap(__builtin_bit_cast(unsigned, a), __builtin_bit_cast(unsigned, b), false, false);
  return asf(r[0]) + asf(r[1]);
}
DI float sw16_add(float a, float b) {
  auto r = __builtin_amdgcn_permlane16_swap(__builtin_bit_cast(unsigned, a), __builtin_bit_cast(unsigned, b), false, false);
  return asf(r[0]) + asf(r[1]);
}
DI float row_sum(float x) { return dpp_add<DPP_ROR8>(dpp_add<DPP_ROR4>(dpp_add<DPP_X2>(dpp_add<DPP_X1>(x)))); }
DI unsigned ln_pack(float s, float q) {
  const float m = s * (1.0f / D_);
  float v = q * (1.0f / D_) - m * m; v = v > 0.f ? v : 0.f;
  const float sd = __builtin_sqrtf(rbf(rbf(v) + 1e-5f));
  return pkbf(m, sd);
}

DI void mf32_first(f32x16& d, const u32x4& a, const u32x4& b) { asm volatile("v_mfma_f32_32x32x16_bf16 %0, %1, %2, 0" : "=&v"(d) : "a"(a), "v"(b)); }
DI void mf32_acc(f32x16& d, const u32x4& a, const u32x4& b) { asm volatile("v_mfma_f32_32x32x16_bf16 %0, %1, %2, %0" : "+v"(d) : "a"(a), "v"(b)); }
DI void mf32_vv(f32x16& d, const u32x4& a, const u32x4& b) { asm volatile("v_mfma_f32_32x32x16_bf16 %0, %1, %2, %0" : "+v"(d) : "v"(a), "v"(b)); }
DI void mf32_va(f32x16& d, const u32x4& a, const u32x4& b) { asm volatile("v_mfma_f32_32x32x16_bf16 %0, %1, %2, %0" : "+v"(d) : "v"(a), "a"(b)); }
DI void mh32_vv(f32x16& d, const u32x4& a, const u32x4& b) { asm volatile("v_mfma_f32_32x32x16_f16 %0, %1, %2, %0" : "+v"(d) : "v"(a), "v"(b)); }
DI void mf16_a0(f32x4& d, const u32x4& a, const u32x4& b) { asm volatile("v_mfma_f32_16x16x32_bf16 %0, %1, %2, 0" : "=&v"(d) : "a"(a), "v"(b)); }
DI void mf16_a(f32x4& d, const u32x4& a, const u32x4& b) { asm volatile("v_mfma_f32_16x16x32_bf16 %0, %1, %2, %0" : "+v"(d) : "a"(a), "v"(b)); }
DI void mf16_v(f32x4& d, const u32x4& a, const u32x4& b) { asm volatile("v_mfma_f32_16x16x32_bf16 %0, %1, %2, %0" : "+v"(d) : "v"(a), "v"(b)); }

template <int IMM, bool COH> DI void dma(unsigned voff, rsrc_t rs, unsigned soff) {
  if (COH) asm volatile("buffer_load_dwordx4 %0, %1, %2 offen offset:%3 sc1 lds" :: "v"(voff), "s"(rs), "s"(soff), "i"(IMM) : "memory");
  else asm volatile("buffer_load_dwordx4 %0, %1, %2 offen offset:%3 lds" :: "v"(voff), "s"(rs), "s"(soff), "i"(IMM) : "memory");
}
DI void set_m0(unsigned v) { asm volatile("s_mov_b32 m0, %0\n\ts_nop 0" :: "s"(v) : "memory"); }

DI int fresh(int x) { asm volatile("" : "+v"(x)); return x; }
#define STAMP(i) do {} while (0)
#define STAMP_DECL do {} while (0)
#define STAMP_OUT() do {} while (0)

#define VMCLEAN() do { VMWAIT(0); __builtin_amdgcn_s_waitcnt(0x0F70); } while (0)
#define RELOAD() asm volatile("" ::: "memory")
DI void poll_flags(unsigned char* ws, unsigned off, bool active, unsigned want, unsigned win) {
  if (active) {
    #pragma unroll 1
    for (unsigned n = 0; n < SPIN_CAP; ++n)
      if ((ld_flag(ws, off) - want) < win) break;
  }
  VMCLEAN();
}

DI void helper_main(const Params& p, lptr lds, unsigned tagb, int xg, int hp, int lane, int wv) {
  const rsrc_t rws = mk_rsrc(p.ws, WS_TOTAL);
  {
    const int hid = 4 * xg + hp, tid = 64 * wv + lane, co = tid % 14, rg = tid / 14;
    const rsrc_t rw = mk_rsrc(hid < 16 ? p.wg : p.wu, D_ * F_ * 2);
    const unsigned vo = rg < 16 ? rg * 28 * F_ * 2 + ((hid & 15) * 112 + co * 8) * 2 : 0xfffffff0u;
    f32x2 a[4] = {};
    #pragma unroll 1
    for (int r0 = 0; r0 < 28; r0 += 14) {
      u32x4 v[14];
      #pragma unroll
      for (int r = 0; r < 14; ++r) v[r] = __builtin_amdgcn_raw_buffer_load_b128(rw, vo, (r0 + r) * F_ * 2, 0);
      #pragma unroll
      for (int r = 0; r < 14; ++r)
        #pragma unroll
        for (int d = 0; d < 4; ++d) a[d] += f32x2{bflo(v[r][d]), bfhi(v[r][d])};
    }
    if (rg < 16) {
      *(LAS f32x4*)(lds + rg * 448 + co * 32) = f32x4{a[0].x, a[0].y, a[1].x, a[1].y};
      *(LAS f32x4*)(lds + rg * 448 + co * 32 + 16) = f32x4{a[2].x, a[2].y, a[3].x, a[3].y};
    }
    BAR();
    if (tid < 112) {
      float t = 0.f;
      #pragma unroll
      for (int r = 0; r < 16; ++r) t += *(LAS float*)(lds + r * 448 + tid * 4);
      st_gran(p.ws, WS_CS + (hid * 112 + tid) * 8, __builtin_bit_cast(unsigned, t), tagb + T_CS);
    }
  }
  #pragma unroll 1
  for (int layer = 0; layer < 2; ++layer) {
    poll_flags(p.ws, WS_FX + (56 * wv + lane) * 64, lane < 56, tagb + T_FX + layer, 2);
    u32x4 v[4][4];
    const unsigned s0 = 4 * xg + hp;
    const unsigned vo = ((s0 >> 1) * D_ + 112 * wv + (lane >> 1)) * 64 + (s0 & 1) * 32 + (lane & 1) * 16;
    #pragma unroll
    for (int q = 0; q < 4; ++q)
      #pragma unroll
      for (int i = 0; i < 4; ++i) {
        const unsigned o = (i == 3 && lane >= 32) ? 0xfffffff0u : vo + i * 2048;
        v[q][i] = __builtin_amdgcn_raw_buffer_load_b128(rws, o, WS_X1 + layer * ACT_BYTES + q * 16 * D_ * 64, SC1);
      }
    #pragma unroll
    for (int q = 0; q < 4; ++q) {
      f32x2 s[4] = {}, sq[4] = {};
      #pragma unroll
      for (int i = 0; i < 4; ++i)
        #pragma unroll
        for (int d = 0; d < 4; ++d) { f32x2 x = {bflo(v[q][i][d]), bfhi(v[q][i][d])}; s[d] += x; sq[d] = __builtin_elementwise_fma(x, x, sq[d]); }
      float r[8], w[4];
      #pragma unroll
      for (int d = 0; d < 4; ++d) { r[2 * d] = sw32_add(s[d].x, sq[d].x); r[2 * d + 1] = sw32_add(s[d].y, sq[d].y); }
      #pragma unroll
      for (int d = 0; d < 4; ++d) w[d] = dpp_add<DPP_ROR8>(dpp_add<DPP_ROR4>(dpp_add<DPP_X2>(sw16_add(r[2 * d], r[2 * d + 1]))));
      if ((lane & 14) == 0) {
        LAS float* o = (LAS float*)(lds + LA_HS + (q & 1) * 512 + wv * 128 + (lane >> 5) * 64) + 8 * (lane & 1) + ((lane >> 4) & 1);
        #pragma unroll
        for (int d = 0; d < 4; ++d) o[2 * d] = w[d];
      }
      BAR();
      if (wv == q && lane < 16) {
        LAS float* o = (LAS float*)(lds + LA_HS + (q & 1) * 512) + lane;
        const float a = (o[0] + o[32]) + (o[64] + o[96]), b = (o[16] + o[48]) + (o[80] + o[112]);
        st_gran(p.ws, WS_ST + (2 * layer + 1) * 16384 + ((32 * q + s0) * 16 + lane) * 8, ln_pack(a, b), tagb + T_ST + 2 * layer + 1);
      }
    }
    if (layer == 0) {
      const unsigned S = 32 * wv + s0;
      u32x2 g[4];
      const unsigned go = ((S * 7 + (lane >> 5)) * 32 + (lane & 31)) * 8;
      #pragma unroll 1
      for (unsigned n = 0; n < SPIN_CAP; ++n) {
        RELOAD();
        #pragma unroll
        for (int i = 0; i < 4; ++i) g[i] = __builtin_amdgcn_raw_buffer_load_b64(rws, (i == 3 && lane >= 32) ? 0xfffffff0u : go + i * 512, WS_PS, SC1);
        const unsigned tg = tagb + T_PS;
        if (g[0].y == tg && g[1].y == tg && g[2].y == tg && (lane >= 32 || g[3].y == tg)) break;
      }
      float acc = (asf(g[0].x) + asf(g[1].x)) + (asf(g[2].x) + asf(g[3].x));
      acc = sw32_add(acc, acc);
      const float other = __builtin_bit_cast(float, __builtin_amdgcn_update_dpp(0, __builtin_bit_cast(int, acc), DPP_X1, 0xf, 0xf, true));
      if (lane < 32 && !(lane & 1)) st_gran(p.ws, WS_ST + 2 * 16384 + (S * 16 + (lane >> 1)) * 8, ln_pack(acc, other), tagb + T_ST + 2);
    }
    BAR();
  }
}

constexpr int G2_ORD[14] = {0, 1, 9, 2, 10, 3, 11, 4, 12, 5, 13, 6, 7, 8};
struct Wts { u32x4 W1[56]; u32x4 WdA[8]; u32x4 WdV[28]; u32x4 WS[2]; };

struct Rot { int a, b, st; DI int k(int o) const { return (o < 7 ? a : b) + (o % 7) * st; } };
DI u32x4 wd_load(rsrc_t rwd, unsigned vo, int kstep, int j) { return __builtin_amdgcn_raw_buffer_load_b128(rwd, vo + j * 896, kstep * 32 * 896, 0); }
DI void wd_conv(const u32x4* r, u32x4* f) {
  unsigned lo[4][2], hi[4][2];
  #pragma unroll
  for (int j = 0; j < 4; ++j)
    #pragma unroll
    for (int i = 0; i < 2; ++i) {
      const unsigned a = r[j][i], b = r[j][2 + i];
      lo[j][i] = (unsigned)__builtin_amdgcn_update_dpp((int)a, (int)b, DPP_ROR8, 0xf, 0xc, false);
      hi[j][i] = (unsigned)__builtin_amdgcn_update_dpp((int)b, (int)a, DPP_ROR8, 0xf, 0x3, false);
    }
  #pragma unroll
  for (int t = 0; t < 4; ++t) {
    const unsigned sel = (t & 1) ? 0x07060302u : 0x05040100u;
    f[t] = u32x4{__builtin_amdgcn_perm(lo[1][t >> 1], lo[0][t >> 1], sel), __builtin_amdgcn_perm(lo[3][t >> 1], lo[2][t >> 1], sel),
                 __builtin_amdgcn_perm(hi[1][t >> 1], hi[0][t >> 1], sel), __builtin_amdgcn_perm(hi[3][t >> 1], hi[2][t >> 1], sel)};
  }
  asm volatile("" : "+v"(f[0]), "+v"(f[1]), "+v"(f[2]), "+v"(f[3]));
  GAP();
}
DI void w1_piece(rsrc_t rw, unsigned vo, const unsigned (&gb)[4], unsigned m0w, int k0, int i) {
  const int slot = k0 + (i >> 1);
  asm volatile("s_mov_b32 m0, %0" :: "s"(m0w + i * 2048) : "memory");
  const unsigned so = gb[slot / 7] + ((slot % 7) * 16 + (i & 1) * 8) * F_ * 2;
  asm volatile("s_nop 0\n\tbuffer_load_dwordx4 %0, %1, %2 offen lds" :: "v"(vo), "s"(rw), "s"(so) : "memory");
}
template <int K0, int NK> DI void w1_read(Wts& w, lptr lds, unsigned rb) {
  #pragma unroll
  for (int s = 0; s < NK; ++s)
    #pragma unroll
    for (int t = 0; t < 2; ++t) {
      lptr a = lds + (rb ^ (t * 32)) + s * 4096;
      w.W1[2 * (K0 + s) + t] = cat(trr(a), trr(a + 1024));
    }
}
template <int K0, int NK> DI void w1_pin(Wts& w) {
  #pragma unroll
  for (int s = 2 * K0; s < 2 * (K0 + NK); ++s) asm volatile("" : "+a"(w.W1[s]));
}

DI void kt_build(lptr lds, int tid) {
  #pragma unroll
  for (int ch = 0; ch < 2; ++ch) {
    const u32x4 a = *(LAS u32x4*)(lds + LA_KC + ch * 4160 + tid * 16);
    const u32x2 b = *(LAS u32x2*)(lds + LA_KC + ch * 4160 + tid * 16 + 16);
    const unsigned e0 = __builtin_amdgcn_alignbyte(a.y, a.x, 2), e1 = __builtin_amdgcn_alignbyte(a.z, a.y, 2),
                   e2 = __builtin_amdgcn_alignbyte(a.w, a.z, 2), e3 = __builtin_amdgcn_alignbyte(b.x, a.w, 2),
                   e4 = __builtin_amdgcn_alignbyte(b.y, b.x, 2);
    lptr o = lds + LA_KT + ch * KT_CH + tid * 16;
    *(LAS u32x4*)(o) = a;
    *(LAS u32x4*)(o + KT_CP) = u32x4{e0, e1, e2, e3};
    *(LAS u32x4*)(o + 2 * KT_CP) = u32x4{a.y, a.z, a.w, b.x};
    *(LAS u32x4*)(o + 3 * KT_CP) = u32x4{e1, e2, e3, e4};
  }
  const u32x4 z = {0, 0, 0, 0};
  if (tid < 32) *(LAS u32x4*)(lds + LA_KT + (tid >> 2) * KT_CP + 4096 + (tid & 3) * 16) = z;
  if (tid < 248) { const int ch = tid >= 124, i = tid - 124 * ch; *(LAS u32x4*)(lds + LA_U + ch * U_CH + (i >> 2) * 80 + (i & 3) * 16) = z; }
}

struct CFrag { u32x4 lo, hi, bl, bh; };
DI u32x4 ld2x64(lptr p) { return cat(*(volatile LAS u32x2*)(p), *(volatile LAS u32x2*)(p + 8)); }
template <class F> DI void conv_half(f32x16& a0, f32x16& a1, const CFrag& c, CFrag& n, lptr pa, lptr pb, F&& ride) {
  mh32_vv(a1, c.hi, c.bl); GAP(); n.hi = ld2x64(pa); GAP();
  mh32_vv(a0, c.lo, c.bl); GAP(); n.bl = *(LAS u32x4*)(pb); n.bh = *(LAS u32x4*)(pb + 2560); GAP(); ride(); GAP();
  mh32_vv(a1, c.lo, c.bh); GAP(); n.lo = ld2x64(pa + 2048); GAP();
}

template <int Q, class F>
DI void g1_step(const Wts& w, lptr lds, f32x16 (&cur)[2], f32x16 (&prev)[2], u32x4 (&B)[3], unsigned rd_cur, unsigned rd_next,
                float c1, float sd, unsigned negm, rsrc_t rws, unsigned hvo, unsigned hso,
                unsigned xvo, unsigned m0base, unsigned xso,
                u32x2& stn, unsigned stvo, unsigned stso, unsigned char* ws, unsigned fhoff, unsigned fhval, int lane, F&& ride) {
  constexpr bool EPI = Q > 0, MMA = Q < 4, DMA = Q == 1 || Q == 2;
  float e[16]; unsigned pk[8];
  auto gidx = [](int i) { return ((i >> 2) & 1) * 8 + (i & 3); };
  constexpr int E0 = 8;
  if (EPI && !MMA) { u32x4 bm = {negm, negm, negm, negm}; asm volatile("s_nop 1" : "+v"(bm)); mf32_vv(prev[0], w.WS[0], bm); mf32_vv(prev[1], w.WS[1], bm);
    asm volatile("s_nop 11" : "+v"(prev[0]), "+v"(prev[1])); }
  #pragma unroll
  for (int s = MMA ? 0 : E0; s < 28; ++s) {
    if (MMA) {
      if (s == 0) mf32_first(cur[0], w.W1[0], B[0]); else mf32_acc(cur[0], w.W1[2 * s], B[s % 3]);
      GAP();
      const int sp = s + 2; lptr a = lds + (sp < 28 ? rd_cur + sp * 1024 : rd_next + (sp - 28) * 1024);
      if (sp < 28 || Q < 3) B[sp % 3] = cat(trr(a), trr(a + 256));
      GAP();
      if (EPI && s == 0) { u32x4 bm = {negm, negm, negm, negm}; asm volatile("s_nop 1" : "+v"(bm)); mf32_vv(prev[0], w.WS[0], bm); mf32_vv(prev[1], w.WS[1], bm); }
    }
    if (EPI) {
      const int i = s - E0;
      if (i - 1 >= 0 && i - 1 < 16) e[i - 1] = __builtin_amdgcn_exp2f(e[i - 1]);
      if (i >= 0 && i < 16) e[i] = prev[i >> 3][gidx(i)] * c1;
      if (i - 2 >= 0 && i - 2 < 16) e[i - 2] = __builtin_fmaf(e[i - 2], sd, sd);
    }
    GAP();
    if (MMA) {
      if (s == 0) mf32_first(cur[1], w.W1[1], B[0]); else mf32_acc(cur[1], w.W1[2 * s + 1], B[s % 3]);
      GAP();
    }
    if (EPI) {
      const int i = s - E0;
      if (i - 2 >= 0 && i - 2 < 16) e[i - 2] = __builtin_amdgcn_rcpf(e[i - 2]);
      if (i - 3 >= 0 && i - 3 < 16) e[i - 3] = prev[(i - 3) >> 3][gidx(i - 3) + 4] * e[i - 3];
      const int j = i - 4;
      if (j >= 0 && j < 16 && (j & 1)) pk[j >> 1] = pkbf_s(e[j - 1], e[j]);
      if (j == 8) __builtin_amdgcn_raw_buffer_store_b128(u32x4{pk[0], pk[1], pk[2], pk[3]}, rws, hvo, hso, SC1);
      if (j == 15) { GAP(); __builtin_amdgcn_raw_buffer_store_b128(u32x4{pk[4], pk[5], pk[6], pk[7]}, rws, hvo + 512, hso, SC1); }
    }
    GAP(); ride(s); GAP();
    if (MMA) {
      if (DMA) {
        if (s == 0) { asm volatile("s_barrier" ::: "memory");
                      set_m0(m0base); dma<0, true>(xvo, rws, xso); }
        if (s == 1) dma<1024, true>(xvo, rws, xso);
        if (s == 2) dma<2048, true>(xvo, rws, xso);
        if (s == 3) dma<3072, true>(xvo, rws, xso);
        if (s == 4) { set_m0(m0base + 4096); dma<0, true>(xvo, rws, xso + 4096); }
        if (s == 5) dma<1024, true>(xvo, rws, xso + 4096);
        if (s == 6) dma<2048, true>(xvo, rws, xso + 4096);
      }
      if (s == 14 && Q >= 2) {
        if (Q == 2) VMWAIT(7); else VMWAIT(0);
        if (lane == 0) st_flag(ws, fhoff, fhval);
      }
      if (s == 16) stn = __builtin_amdgcn_raw_buffer_load_b64(rws, stvo, stso, SC1);
      if (s == 25 && Q == 0) asm volatile("s_waitcnt vmcnt(1)\n\ts_barrier" ::: "memory");
      if (s == 25 && Q == 1) asm volatile("s_waitcnt vmcnt(2)\n\ts_barrier" ::: "memory");
      if (s == 25 && Q == 2) asm volatile("s_waitcnt vmcnt(3)\n\ts_barrier" ::: "memory");
      GAP();
    }
  }
}

DI void stats_take(u32x2& g, rsrc_t rws, unsigned vo, unsigned so, unsigned tag, float& c1, float& sd, unsigned& negm) {
  asm volatile("" : "+v"(g));
  #pragma unroll 1
  for (unsigned n = 0; n < SPIN_CAP && g.y != tag; ++n) { RELOAD(); g = __builtin_amdgcn_raw_buffer_load_b64(rws, vo, so, SC1); }
  sd = bfhi(g.x);
  c1 = -1.4426950408889634f * __builtin_amdgcn_rcpf(sd);
  const unsigned nm = (g.x & 0xffffu) ^ 0x8000u;
  negm = nm | (nm << 16);
}

__global__ void __launch_bounds__(256, 1) fused_kernel(Params p) {
  extern __shared__ __attribute__((aligned(16))) unsigned char smem[];
  const lptr lds = (lptr)smem;
  const int tid = threadIdx.x, lane = tid & 63, wv = __builtin_amdgcn_readfirstlane(tid >> 6);
  const int b = blockIdx.x, xg = b & 7, idx = b >> 3;
  const bool worker = idx < 28;
  STAMP_DECL;
  STAMP(0);
  const rsrc_t rws = mk_rsrc(p.ws, WS_TOTAL);

  const unsigned epoch = ld_flag(p.ws, WS_EPOCH);
  const rsrc_t ru = mk_rsrc(p.u, ACT_BYTES);
  u32x4 xr[2];
  #pragma unroll
  for (int i = 0; i < 2; ++i) xr[i] = __builtin_amdgcn_raw_buffer_load_b128(ru, lane < 56 ? lane * 16 : 0xfffffff0u, (8 * b + 2 * wv + i) * 896, 0);
  const int c0 = worker ? 56 * xg + 2 * idx : 0;
  const int kch = c0 + (wv >> 1);
  unsigned pr[2][4];
  #pragma unroll
  for (int it = 0; it < 2; ++it) {
    const int o = kch * N_ + (wv & 1) * 128 + it * 64 + lane;
    pr[it][0] = p.lar[o]; pr[it][1] = p.aim[o]; pr[it][2] = p.cre[o]; pr[it][3] = p.cim[o];
  }
  const unsigned ldt2 = *(const unsigned*)(p.ldt + c0), dsk2 = *(const unsigned*)(p.dsk + c0);
  const int sid = idx & 3;
  const int gam = 4 * (xg & 3) + sid, js = 7 * (xg >> 2) + (idx >> 2);
  const int s2 = 4 * xg + sid, cc = idx >> 2;
  const Rot rot = {14 * wv + ((sid & 1) ? 7 : 0) + ((sid & 2) ? 6 : 0), 14 * wv + ((sid & 1) ? 0 : 7) + ((sid & 2) ? 6 : 0), (sid & 2) ? -1 : 1};
  const rsrc_t rwd = mk_rsrc(p.wd, worker ? D_ * F_ * 2 : 0);
  const unsigned wdvo = (16 * (lane >> 5) + 4 * ((lane >> 4) & 1) + 8 * ((lane >> 3) & 1)) * 896 + (lane & 7) * 16 + cc * 128;
  u32x4 rawA[16];
  {
    unsigned long long pc; asm volatile("s_getpc_b64 %0" : "=s"(pc));
    const rsrc_t rc = mk_rsrc((const void*)(pc & ~255ull), CODE_PREFETCH);
    set_m0(LA_DUMP);
    #pragma unroll
    for (int i = 0; i < 3; ++i) dma<0, false>(lane * 16, rc, (i * 16 + (idx & 3) * 4 + wv) * 1024);
  }
  unsigned kidx[8];
  #pragma unroll
  for (int j = 0; j < 8; ++j) {
    const int l = 2047 - 8 * tid - j;
    const unsigned pos = (unsigned)rbf((float)l), hb = pos >> 8;
    const unsigned sh = hb >= 4 ? 3u : (hb >= 2 ? 2u : hb);
    const unsigned base = (unsigned)((0x14ull << 40 | 0x13ull << 35 | 0x12ull << 30 | 0x11ull << 25 | 0x10ull << 20 | 0xeull << 15 | 0xcull << 10 | 0x8ull << 5) >> (5 * hb)) & 31u;
    kidx[j] = ((base + ((pos & 7u) >> sh)) * 36 + ((pos & 255u) >> 3)) * 4;
    asm volatile("" : "+v"(kidx[j]));
  }
  GAP();
  const unsigned ep = __builtin_amdgcn_readfirstlane(epoch), tagb = (ep + 1u) * 0x9E3779B1u;

  unsigned st1;
  {
    float s[2], q[2];
    #pragma unroll
    for (int i = 0; i < 2; ++i) {
      f32x2 a = {0.f, 0.f}, c = {0.f, 0.f};
      #pragma unroll
      for (int d = 0; d < 4; ++d) { f32x2 x = {bflo(xr[i][d]), bfhi(xr[i][d])}; a += x; c = __builtin_elementwise_fma(x, x, c); }
      s[i] = a.x + a.y; q[i] = c.x + c.y;
      if (lane < 56) *(LAS u32x4*)(lds + LA_KC + (2 * wv + i) * 896 + lane * 16) = xr[i];
    }
    float r = row_sum(sw16_add(sw32_add(s[0], q[0]), sw32_add(s[1], q[1])));
    const float o = asf(__builtin_amdgcn_permlane32_swap(__builtin_bit_cast(unsigned, r), __builtin_bit_cast(unsigned, r), false, false)[1]);
    st1 = ln_pack(r, o);
    BAR();
    #pragma unroll
    for (int i = 0; i < 4; ++i) {
      const int g = 4 * i + (lane >> 4), cg = (wv & 1) * 14 + (g < 14 ? g : 0);
      const u32x2 t = trr(lds + LA_KC + (4 * (wv >> 1) + ((lane >> 2) & 3)) * 896 + cg * 32 + (lane & 3) * 8);
      __builtin_amdgcn_raw_buffer_store_b64(t, rws, g < 14 ? (cg * 16 + (lane & 15)) * 8 : 0xfffffff0u, WS_XT + (2 * b + (wv >> 1)) * D_ * 8, SC1);
    }
  }
  asm volatile("" : "+v"(pr[0][0]), "+v"(pr[0][1]), "+v"(pr[0][2]), "+v"(pr[0][3]), "+v"(pr[1][0]), "+v"(pr[1][1]), "+v"(pr[1][2]), "+v"(pr[1][3]));
  const unsigned st1off = WS_ST + (8 * b + 2 * wv + (lane >> 4)) * 8;
  if (!worker) {
    VMWAIT(0); BAR();
    if (lane == 0 || lane == 16) st_gran(p.ws, st1off, st1, tagb + T_ST);
    helper_main(p, lds, tagb, xg, idx - 28, lane, wv);
    return;
  }
  STAMP(1);

  Wts w;
  const rsrc_t rw1 = mk_rsrc(wv < 2 ? p.wg : p.wu, D_ * F_ * 2);
  const unsigned w1vo = (4 * (wv & 1) + (lane >> 4)) * F_ * 2 + (((lane & 15) ^ ((2 * (lane >> 4) + (wv >> 1)) << 1)) * 16);
  const unsigned w1m0 = LA_W + (wv >> 1) * W_PL + (wv & 1) * 1024;
  unsigned w1gb[4];
  #pragma unroll
  for (int g = 0; g < 4; ++g) w1gb[g] = ((g + sid) & 3) * 112 * F_ * 2 + js * 256;
  const unsigned w1rb = LA_W + ((lane >> 1) & 1) * W_PL + (8 * (lane >> 5) + ((lane >> 2) & 3)) * 256 +
                        (((4 * wv + ((lane >> 4) & 1)) ^ ((2 * ((lane >> 2) & 3) + ((lane >> 1) & 1)) << 1)) * 16) + (lane & 1) * 8;

  const float dt0 = rbf(expf(bflo(ldt2))), dt1 = rbf(expf(bfhi(ldt2)));
  float ysc0, ysc1;
  {
    const float dt = (wv >> 1) ? dt1 : dt0;
    unsigned mx = max(max(pr[0][2] & 0x7fffu, pr[0][3] & 0x7fffu), max(pr[1][2] & 0x7fffu, pr[1][3] & 0x7fffu));
    mx = dpp_umax<DPP_ROR8>(dpp_umax<DPP_ROR4>(dpp_umax<DPP_X2>(dpp_umax<DPP_X1>(mx))));
    unsigned eb = max(max(__builtin_amdgcn_readlane(mx, 0), __builtin_amdgcn_readlane(mx, 16)), max(__builtin_amdgcn_readlane(mx, 32), __builtin_amdgcn_readlane(mx, 48))) >> 7;
    eb = min(max(eb, 1u), 253u);
    const float cs = __builtin_bit_cast(float, (254u - eb) << 23);
    f32x16 acc = {};
    const lptr tb = lds + wv * TB_W;
    STAMP(2);
    #pragma unroll
    for (int it = 0; it < 2; ++it) {
      auto ride = [&](int n) { w1_piece(rw1, w1vo, w1gb, w1m0, 0, 10 * it + n); };
      const float lr = -rbf(expf(bflo(pr[it][0]))) * dt * 1.4426950408889634f;
      const float li = bflo(pr[it][1]) * dt * 0.15915494309189535f;
      auto zpow = [&](float pw) { const float m = __builtin_amdgcn_exp2f(lr * pw); float t = li * pw; t -= __builtin_rintf(t);
                                  return f32x2{m * __builtin_amdgcn_cosf(t), m * __builtin_amdgcn_sinf(t)}; };
      const f32x2 z8 = zpow(8.f), y1 = zpow(1.f), yb = zpow(256.f);
      const f32x2 z8r = {-z8.y, z8.x};
      const f32x2 z1 = {y1.x, -y1.y}, z1r = {y1.y, y1.x}, zb = {yb.x, -yb.y}, zbr = {yb.y, yb.x};
      volatile LAS unsigned* o = (volatile LAS unsigned*)(tb + lane * 4);
      f32x2 x = {1.f, 0.f}, v[21];
      v[0] = f32x2{bflo(pr[it][2]) * cs, -bflo(pr[it][3]) * cs};
      GAP();
      #pragma unroll
      for (int r = 0; r < 32; ++r) {
        const int c = r + 1, sc = c < 8 ? c - 1 : c < 12 ? 2 * (c - 8) : c == 12 ? 8 : c == 13 ? 10 : c < 17 ? c - 2 : c - 1;
        f32x2 tx, tv; unsigned px, pv;
        px = pkh(x.x, x.y); GAP();
        if (r < 31) { tx = x.xx * z8; GAP(); }
        o[r * 68] = px; GAP();
        if (r < 21) { pv = pkh(v[r].x, v[r].y); GAP(); }
        if (r < 31) { x = __builtin_elementwise_fma(x.yy, z8r, tx); GAP(); }
        if (c < 21) { tv = v[sc].xx * (c < 8 ? z1 : zb); GAP(); }
        if (r < 21) { o[(32 + r) * 68] = pv; GAP(); }
        if (c < 21) { v[c] = __builtin_elementwise_fma(v[sc].yy, c < 8 ? z1r : zbr, tv); GAP(); }
        if (r % 3 == 0 && r < 30) { ride(r / 3); GAP(); }
        if (it == 1 && (r & 1)) { rawA[r >> 1] = wd_load(rwd, wdvo, rot.k(r >> 3), (r >> 1) & 3); GAP(); }
        if (it == 0 && r == 28) {
          asm volatile("s_waitcnt vmcnt(10)\n\ts_barrier" ::: "memory");
          if (lane == 0 || lane == 16) st_gran(p.ws, st1off, st1, tagb + T_ST);
        }
      }
      const lptr ra = tb + (lane & 31) * 272 + (lane >> 5) * 16, rb = tb + (32 + min(lane & 31, 20)) * 272 + (lane >> 5) * 16;
      u32x4 fa[8], fb[8];
      #pragma unroll
      for (int s = 0; s < 8; ++s) { fa[s] = *(LAS u32x4*)(ra + s * 32); fb[s] = *(LAS u32x4*)(rb + s * 32); }
      asm volatile("s_nop 1" : "+v"(acc));
      #pragma unroll
      for (int s = 0; s < 8; ++s) mh32_vv(acc, fa[s], fb[s]);
      STAMP(3 + it);
    }
    asm volatile("s_nop 11" : "+v"(acc));
    if ((lane & 31) < 21) {
      #pragma unroll
      for (int i = 0; i < 4; ++i)
        *(LAS f32x4*)(lds + LA_RED + ((wv * 21 + (lane & 31)) * 36 + 8 * i + 4 * (lane >> 5)) * 4) = f32x4{acc[4 * i], acc[4 * i + 1], acc[4 * i + 2], acc[4 * i + 3]};
    }
    if (lane == 0) *(LAS unsigned*)(lds + LA_MISC + wv * 4) = eb;
    BAR();
    STAMP(5);
    const u32x4 e4 = *(LAS u32x4*)(lds + LA_MISC);
    #pragma unroll
    for (int ch = 0; ch < 2; ++ch) {
      const unsigned ea = e4[2 * ch], ebb = e4[2 * ch + 1], em = max(ea, ebb);
      const float fa = em - ea > 120u ? 0.f : __builtin_bit_cast(float, (132u - (em - ea)) << 23);
      const float fb = em - ebb > 120u ? 0.f : __builtin_bit_cast(float, (132u - (em - ebb)) << 23);
      float k[8];
      #pragma unroll
      for (int j = 0; j < 8; ++j)
        k[j] = *(LAS float*)(lds + LA_RED + (2 * ch) * 3024 + kidx[j]) * fa + *(LAS float*)(lds + LA_RED + (2 * ch + 1) * 3024 + kidx[j]) * fb;
      *(LAS u32x4*)(lds + LA_KC + ch * 4160 + tid * 16) = u32x4{pkh(k[0], k[1]), pkh(k[2], k[3]), pkh(k[4], k[5]), pkh(k[6], k[7])};
      const float ys = (ch ? dt1 : dt0) * __builtin_bit_cast(float, em << 23) * 0.0625f;
      if (ch) ysc1 = ys; else ysc0 = ys;
    }
    if (tid < 8) *(LAS u32x4*)(lds + LA_KC + (tid >> 2) * 4160 + 4096 + (tid & 3) * 16) = u32x4{0, 0, 0, 0};
    BAR();
  }
  STAMP(6);
  const float dk0 = bflo(dsk2), dk1 = bfhi(dsk2);

  float base[2][8];
  auto conv_in = [&](const int layer, auto&& meanwhile) {
    const int tid = fresh(threadIdx.x);
    u32x4 g[4]; unsigned fa, fb;
    const unsigned stso = WS_ST + 2 * layer * 16384, tag = tagb + T_ST + 2 * layer;
    const unsigned f2off = WS_F2 + ((((tid >> 1) & 31) * 7 + (c0 >> 6)) * 4 + 2 * (tid & 1)) * 64, f2want = tagb + T_F2 + wv + 1;
    auto fetch = [&]() {
      fa = fb = f2want;
      if (layer) { fa = __builtin_amdgcn_raw_buffer_load_b32(rws, f2off, 0, SC1); fb = __builtin_amdgcn_raw_buffer_load_b32(rws, f2off + 64, 0, SC1); }
      #pragma unroll
      for (int i = 0; i < 4; ++i) g[i] = __builtin_amdgcn_raw_buffer_load_b128(rws, tid * 64 + i * 16, stso, SC1);
      GAP();
    };
    if (layer == 0) fetch();
    kt_build(lds, tid);
    STAMP(7 + 24 * layer);
    if (layer) fetch();
    #pragma unroll 1
    for (unsigned n = 0; n < SPIN_CAP; ++n) {
      bool ok = (fa - f2want) < 4u && (fb - f2want) < 4u;
      #pragma unroll
      for (int i = 0; i < 4; ++i) ok = ok && g[i].y == tag && g[i].w == tag;
      if (ok) break;
      RELOAD();
      fetch();
    }
    VMCLEAN();
    STAMP(8 + 24 * layer);
    u32x4 xa[2];
    #pragma unroll
    for (int i = 0; i < 2; ++i) xa[i] = __builtin_amdgcn_raw_buffer_load_b128(rws, ((2 * tid + i) * D_ + c0) * 8, WS_XT + layer * ACT_BYTES, SC1);
    meanwhile();
    STAMP(9 + 24 * layer);
    float mu[8], rs[8];
    #pragma unroll
    for (int j = 0; j < 8; ++j) { const unsigned v = (j & 1) ? g[j >> 1].z : g[j >> 1].x; mu[j] = bflo(v); rs[j] = __builtin_amdgcn_rcpf(bfhi(v)); }
    #pragma unroll
    for (int ch = 0; ch < 2; ++ch) {
      float u[8];
      #pragma unroll
      for (int j = 0; j < 8; ++j) {
        const unsigned wd = xa[j >> 2][2 * ch + ((j >> 1) & 1)];
        const float x = (j & 1) ? bfhi(wd) : bflo(wd);
        u[j] = (x - mu[j]) * rs[j];
        base[ch][j] = __builtin_fmaf(u[j], ch ? dk1 : dk0, x);
      }
      *(LAS u32x4*)(lds + LA_U + ch * U_CH + ((tid >> 2) + 31) * 80 + (tid & 3) * 16) = u32x4{pkh(u[0], u[1]), pkh(u[2], u[3]), pkh(u[4], u[5]), pkh(u[6], u[7])};
    }
    BAR();
  };
  auto conv_main = [&](const int layer, auto&& ride) {
    STAMP(10 + 24 * layer);
    const int tid = fresh(threadIdx.x), lane = tid & 63;
    const int ch = wv >> 1, d0 = 16 * (wv & 1);
    lptr pa = lds + LA_KT + ch * KT_CH + (3 - (lane & 3)) * KT_CP + 4088 - 2048 - 8 * ((lane & 31) >> 2) + 16 * (lane >> 5) - 64 * d0;
    lptr pb = lds + LA_U + ch * U_CH + ((lane & 31) - d0 + 31) * 80 + 16 * (lane >> 5);
    f32x16 a0 = {}, a1 = {};
    CFrag P, Q;
    P.hi = ld2x64(pa); P.bl = *(LAS u32x4*)(pb); P.lo = ld2x64(pa + 2048); P.bh = *(LAS u32x4*)(pb + 2560);
    asm volatile("s_nop 1" : "+v"(a0), "+v"(a1));
    auto body = [&](int it) {
      conv_half(a0, a1, P, Q, pa + 32, pb + 32, [&]() { ride(2 * it); });
      pa -= 64; pb -= 80;
      conv_half(a0, a1, Q, P, pa, pb, [&]() { ride(2 * it + 1); });
    };
    if (layer == 0) {
      #pragma unroll
      for (int it = 0; it < 16; ++it) body(it);
    } else {
      #pragma unroll 2
      for (int it = 0; it < 16; ++it) body(it);
    }
    STAMP(11 + 24 * layer);
    asm volatile("s_nop 9\n\ts_waitcnt lgkmcnt(0)\n\ts_barrier" : "+v"(a0), "+v"(a1) :: "memory");
    lptr py = lds + LA_Y + ((wv & 1) * 2 + ch) * 9216 + (lane & 31) * 144 + 16 * (lane >> 5);
    #pragma unroll
    for (int i = 0; i < 4; ++i) *(LAS f32x4*)(py + 32 * i) = f32x4{a0[4 * i], a0[4 * i + 1], a0[4 * i + 2], a0[4 * i + 3]};
    #pragma unroll
    for (int i = 0; i < 4; ++i) *(LAS f32x4*)(py + 4608 + 32 * i) = f32x4{a1[4 * i], a1[4 * i + 1], a1[4 * i + 2], a1[4 * i + 3]};
    BAR();
    STAMP(12 + 24 * layer);
    u32x4 o[2];
    #pragma unroll
    for (int c = 0; c < 2; ++c) {
      lptr q = lds + LA_Y + c * 9216 + (tid >> 2) * 144 + (tid & 3) * 32;
      const f32x4 y0 = *(LAS f32x4*)(q) + *(LAS f32x4*)(q + 18432), y1 = *(LAS f32x4*)(q + 16) + *(LAS f32x4*)(q + 18448);
      const float ys = c ? ysc1 : ysc0;
      #pragma unroll
      for (int d = 0; d < 2; ++d) {
        o[c][d] = pkbf(__builtin_fmaf(y0[2 * d], ys, base[c][2 * d]), __builtin_fmaf(y0[2 * d + 1], ys, base[c][2 * d + 1]));
        o[c][2 + d] = pkbf(__builtin_fmaf(y1[2 * d], ys, base[c][4 + 2 * d]), __builtin_fmaf(y1[2 * d + 1], ys, base[c][5 + 2 * d]));
      }
    }
    #pragma unroll
    for (int d = 0; d < 4; ++d) { auto r = __builtin_amdgcn_permlane32_swap(o[0][d], o[1][d], false, false); o[0][d] = r[0]; o[1][d] = r[1]; }
    const unsigned vo = ((16 * wv + ((lane & 31) >> 2)) * D_ + c0 + (lane >> 5)) * 64 + (lane & 3) * 16;
    __builtin_amdgcn_raw_buffer_store_b128(o[0], rws, vo, WS_X1 + layer * ACT_BYTES, SC1);
    __builtin_amdgcn_raw_buffer_store_b128(o[1], rws, vo + 8 * D_ * 64, WS_X1 + layer * ACT_BYTES, SC1);
    STAMP(13 + 24 * layer);
    VMCLEAN(); BAR();
    if (tid == 0) st_flag(p.ws, WS_FX + (28 * xg + idx) * 64, tagb + T_FX + layer);
    STAMP(14 + 24 * layer);
  };
  constexpr unsigned xq = 16 * XB_SZ, hq = 32 * 57344;
  auto g1_begin = [&](const int layer) {
    const int lane = fresh(threadIdx.x) & 63;
    STAMP(15 + 24 * layer);
    poll_flags(p.ws, WS_FX + (56 * ((wv + sid) & 3) + lane) * 64, lane < 56, tagb + T_FX + layer, 2);
    STAMP(16 + 24 * layer);
    const unsigned xso = WS_X1 + layer * ACT_BYTES + gam * XB_SZ + ((wv + sid) & 3) * 7168;
    #pragma unroll
    for (int q = 0; q < 2; ++q) {
      set_m0(q * XB_SZ + wv * 7168);
      dma<0, true>(lane * 16, rws, xso + q * xq); dma<1024, true>(lane * 16, rws, xso + q * xq);
      dma<2048, true>(lane * 16, rws, xso + q * xq); dma<3072, true>(lane * 16, rws, xso + q * xq);
      set_m0(q * XB_SZ + wv * 7168 + 4096);
      dma<0, true>(lane * 16, rws, xso + q * xq + 4096); dma<1024, true>(lane * 16, rws, xso + q * xq + 4096);
      dma<2048, true>(lane * 16, rws, xso + q * xq + 4096);
    }
  };

  {
    conv_in(0, [&]() {
      asm volatile("s_barrier" ::: "memory");
      w1_read<0, 10>(w, lds, w1rb);
      GAP();
      #pragma unroll
      for (int s = 0; s < 4; ++s) {
        u32x4 f[4]; wd_conv(&rawA[4 * s], f);
        #pragma unroll
        for (int t = 0; t < 4; ++t) { if (s < 2) { asm volatile("" : "+a"(f[t])); w.WdA[4 * s + t] = f[t]; } else w.WdV[4 * (s - 2) + t] = f[t]; }
      }
      w1_pin<0, 10>(w);
    });
    if (b == 0 && tid == 0) st_flag(p.ws, WS_EPOCH, ep + 1u);
    u32x4 rawB[16];
    #pragma unroll
    for (int n = 0; n < 4; ++n) rawB[n] = wd_load(rwd, wdvo, rot.k(4), n);
    conv_main(0, [&](int n) { if (n < 20) w1_piece(rw1, w1vo, w1gb, w1m0, 10, n);
                              else rawB[n - 16] = wd_load(rwd, wdvo, rot.k(4 + ((n - 16) >> 2)), n & 3); });
    w1_read<10, 10>(w, lds, w1rb);
    w1_pin<10, 10>(w);
    BAR();
    #pragma unroll
    for (int s = 0; s < 4; ++s) {
      #pragma unroll
      for (int n = 4 * s; n < 4 * s + 4; ++n) w1_piece(rw1, w1vo, w1gb, w1m0, 20, n);
      GAP();
      wd_conv(&rawB[4 * s], &w.WdV[8 + 4 * s]);
    }
    u32x4 rawC[24];
    #pragma unroll
    for (int n = 0; n < 24; ++n) { rawC[n] = wd_load(rwd, wdvo, rot.k(8 + (n >> 2)), n & 3); GAP(); }
    u32x2 cs[2];
    const unsigned csvo = (((lane >> 3) & 1) * F_ + 128 * js + 32 * wv + 8 * ((lane >> 4) & 1) + (lane & 7)) * 8;
    #pragma unroll
    for (int t = 0; t < 2; ++t) cs[t] = __builtin_amdgcn_raw_buffer_load_b64(rws, csvo + t * 128, WS_CS, SC1);
    GAP();
    u32x4 fl[20];
    wd_conv(&rawC[0], &w.WdV[24]);
    #pragma unroll
    for (int s = 0; s < 5; ++s) wd_conv(&rawC[4 + 4 * s], &fl[4 * s]);
    #pragma unroll 1
    for (unsigned n = 0; n < SPIN_CAP && (cs[0].y != tagb + T_CS || cs[1].y != tagb + T_CS); ++n) {
      RELOAD();
      #pragma unroll
      for (int t = 0; t < 2; ++t) cs[t] = __builtin_amdgcn_raw_buffer_load_b64(rws, csvo + t * 128, WS_CS, SC1);
    }
    #pragma unroll
    for (int t = 0; t < 2; ++t) {
      const float a = asf(cs[t].x), h = rbf(a), m = rbf(a - h), l = a - h - m;
      w.WS[t] = lane < 32 ? u32x4{pkbf(h, m), pkbf(l, 0.f), 0, 0} : u32x4{0, 0, 0, 0};
    }
    g1_begin(0);
    asm volatile("s_barrier" ::: "memory");
    w1_read<20, 8>(w, lds, w1rb);
    w1_pin<20, 8>(w);
    BAR();
    #pragma unroll
    for (int i = 0; i < 20; ++i) *(LAS u32x4*)(lds + LA_W + wv * 20480 + i * 1024 + lane * 16) = fl[i];
  }

  #pragma unroll 1
  for (int layer = 0;; ++layer) {
    const unsigned fhb = tagb + T_FH + 4 * layer;
    const int lane = fresh(threadIdx.x) & 63;
    const unsigned fhoff = WS_FH + (gam * 56 + 4 * js + wv) * 64;
    const unsigned fpoll = lane < 14 ? WS_FH + ((s2 >> 1) * 56 + 14 * wv + lane) * 64 : 0xfffffff0u;
    const unsigned hb = WS_H + layer * H_BYTES + s2 * 57344;
    u32x4 Bh[14]; unsigned pf;
    {
      f32x16 A0[2], A1[2];
      const unsigned xso = WS_X1 + layer * ACT_BYTES + gam * XB_SZ + ((wv + sid) & 3) * 7168;
      const unsigned hvo = (((lane >> 4) & 1) * 56 + 4 * js + wv) * 1024 + (16 * (lane >> 5) + (lane & 15)) * 16;
      const unsigned hso = WS_H + layer * H_BYTES + 2 * gam * 57344;
      const unsigned stvo = (32 * gam + (lane & 31)) * 8, stso = WS_ST + (2 * layer + 1) * 16384, sttag = tagb + T_ST + 2 * layer + 1;
      const unsigned rb = (8 * (lane >> 5) + ((lane >> 2) & 3)) * 64 + ((lane >> 4) & 1) * 32 + (lane & 3) * 8;
      u32x4 B[3]; u32x2 stn; float c1 = 0.f, sd = 0.f; unsigned negm = 0;
      auto none = [](int) {};
      asm volatile("s_waitcnt vmcnt(7)\n\ts_barrier" ::: "memory");
      STAMP(17 + 24 * layer);
      #pragma unroll
      for (int s = 0; s < 2; ++s) B[s] = cat(trr(lds + rb + s * 1024), trr(lds + rb + s * 1024 + 256));
      g1_step<0>(w, lds, A0, A1, B, rb, rb + XB_SZ, c1, sd, negm, rws, hvo, hso, lane * 16, 0, 0, stn, stvo, stso, p.ws, fhoff, 0, lane, none);
      { u32x4 t = B[0]; B[0] = B[1]; B[1] = B[2]; B[2] = t; }
      stats_take(stn, rws, stvo, stso, sttag, c1, sd, negm);
      STAMP(18 + 24 * layer);
      g1_step<1>(w, lds, A1, A0, B, rb + XB_SZ, rb, c1, sd, negm, rws, hvo, hso, lane * 16, wv * 7168, xso + 2 * xq, stn, stvo, stso + 4096, p.ws, fhoff, 0, lane, none);
      { u32x4 t = B[0]; B[0] = B[1]; B[1] = B[2]; B[2] = t; }
      stats_take(stn, rws, stvo, stso + 4096, sttag, c1, sd, negm);
      STAMP(19 + 24 * layer);
      g1_step<2>(w, lds, A0, A1, B, rb, rb + XB_SZ, c1, sd, negm, rws, hvo, hso + hq, lane * 16, XB_SZ + wv * 7168, xso + 3 * xq, stn, stvo, stso + 8192, p.ws, fhoff, fhb + 1, lane, none);
      { u32x4 t = B[0]; B[0] = B[1]; B[1] = B[2]; B[2] = t; }
      stats_take(stn, rws, stvo, stso + 8192, sttag, c1, sd, negm);
      STAMP(20 + 24 * layer);
      g1_step<3>(w, lds, A1, A0, B, rb + XB_SZ, rb, c1, sd, negm, rws, hvo, hso + 2 * hq, lane * 16, 0, 0, stn, stvo, stso + 12288, p.ws, fhoff, fhb + 2, lane,
                 [&](int s) { if (s == 15) pf = __builtin_amdgcn_raw_buffer_load_b32(rws, fpoll, 0, SC1); });
      stats_take(stn, rws, stvo, stso + 12288, sttag, c1, sd, negm);
      if (!__all(lane >= 14 || (pf - (fhb + 1)) < 8u)) poll_flags(p.ws, fpoll, lane < 14, fhb + 1, 8);
      STAMP(21 + 24 * layer);
      RELOAD();
      pf = __builtin_amdgcn_raw_buffer_load_b32(rws, fpoll, 0, SC1);
      g1_step<4>(w, lds, A0, A1, B, 0, 0, c1, sd, negm, rws, hvo, hso + 3 * hq, 0, 0, 0, stn, 0, 0, p.ws, 0, 0, lane,
                 [&](int s) { if (s >= 10 && s < 24) Bh[G2_ORD[s - 10]] = __builtin_amdgcn_raw_buffer_load_b128(rws, lane * 16, hb + rot.k(G2_ORD[s - 10]) * 1024, SC1); });
      VMWAIT(17);
      if (lane == 0) st_flag(p.ws, fhoff, fhb + 3);
    }
    STAMP(22 + 24 * layer);
    {
      const unsigned rvo = ((s2 >> 1) * D_ + 64 * cc + lane) * 64 + ((s2 & 1) * 16 + 4 * wv) * 2;
      const lptr wl = lds + LA_W + wv * 20480 + lane * 16;
      const unsigned f2off = WS_F2 + ((s2 * 7 + cc) * 4 + wv) * 64;
      asm volatile("" : "+v"(pf) :: "memory");
      bool ready = __all(lane >= 14 || (pf - (fhb + 2)) < 8u);
      BAR();
      STAMP(23 + 24 * layer);
      #pragma unroll 1
      for (int q = 0; q < 4; ++q) {
        const u32x2 res = __builtin_amdgcn_raw_buffer_load_b64(rws, rvo, WS_X1 + layer * ACT_BYTES + q * xq, SC1);
        const rsrc_t rh = mk_rsrc(p.ws, ready ? WS_TOTAL : 0);
        const unsigned hn = hb + (q + 1) * hq;
        u32x4 wf[4];
        #pragma unroll
        for (int t = 0; t < 4; ++t) wf[t] = *(LAS u32x4*)(wl + t * 1024);
        GAP();
        f32x4 a[4];
        #pragma unroll
        for (int s = 0; s < 14; ++s) {
          const int o = G2_ORD[s];
          #pragma unroll
          for (int t = 0; t < 4; ++t) {
            if (o == 0) mf16_a0(a[t], w.WdA[t], Bh[0]);
            else if (o == 1) mf16_a(a[t], w.WdA[4 + t], Bh[1]);
            else if (o < 9) mf16_v(a[t], w.WdV[4 * (o - 2) + t], Bh[o]);
            else { mf16_v(a[t], wf[t], Bh[o]); if (o < 13) { GAP(); wf[t] = *(LAS u32x4*)(wl + (4 * (o - 8) + t) * 1024); } }
            GAP();
          }
          Bh[o] = __builtin_amdgcn_raw_buffer_load_b128(rh, lane * 16, hn + rot.k(o) * 1024, SC1);
          if (s == 9) pf = __builtin_amdgcn_raw_buffer_load_b32(rws, fpoll, 0, SC1);
          if (s == 8 && q == 0) { VMWAIT(11); if (lane == 0) st_flag(p.ws, fhoff, fhb + 4); }
          GAP();
        }
        if (q == 2) STAMP(56 + 3 * layer);
        asm volatile("s_nop 7" : "+v"(a[0]), "+v"(a[1]), "+v"(a[2]), "+v"(a[3]));
        const lptr pt = lds + (q & 1) * PT_SZ;
        {
          LAS float* o = (LAS float*)(pt + wv * PT_W) + (32 * ((lane >> 4) & 1) + 4 * (lane >> 5)) * 20 + (lane & 15);
          #pragma unroll
          for (int t = 0; t < 4; ++t)
            #pragma unroll
            for (int e = 0; e < 4; ++e) o[(8 * e + t) * 20] = a[t][e];
        }
        BAR();
        if (q == 2) STAMP(57 + 3 * layer);
        const lptr pr2 = pt + lane * 80 + wv * 16;
        const f32x4 y0 = *(LAS f32x4*)(pr2), y1 = *(LAS f32x4*)(pr2 + PT_W), y2 = *(LAS f32x4*)(pr2 + 2 * PT_W), y3 = *(LAS f32x4*)(pr2 + 3 * PT_W);
        GAP();
        const f32x4 y = (y0 + y1) + (y2 + y3);
        const unsigned o0 = pkbf(y[0] + bflo(res.x), y[1] + bfhi(res.x)), o1 = pkbf(y[2] + bflo(res.y), y[3] + bfhi(res.y));
        asm volatile("" : "+v"(pf) :: "memory");
        if (q == 2) STAMP(58 + 3 * layer);
        const bool late = !ready && q < 3;
        ready = __all(lane >= 14 || (pf - (fhb + q + 3)) < 8u) && q < 2;
        const unsigned S = 32 * q + s2;
        if (layer == 0) {
          const float x0 = bflo(o0), x1 = bfhi(o0), x2 = bflo(o1), x3 = bfhi(o1);
          __builtin_amdgcn_raw_buffer_store_b64(u32x2{o0, o1}, rws, (64 * cc + lane) * 8, WS_XT + ACT_BYTES + (4 * S + wv) * D_ * 8, SC1);
          if (q > 0 && lane == 0) st_flag(p.ws, f2off, tagb + T_F2 + q);
          const float w0 = row_sum(sw16_add(sw32_add(x0, x0 * x0), sw32_add(x1, x1 * x1)));
          const float w1 = row_sum(sw16_add(sw32_add(x2, x2 * x2), sw32_add(x3, x3 * x3)));
          if ((lane & 14) == 0)
            st_gran(p.ws, WS_PS + ((S * 7 + cc) * 32 + (4 * wv + 2 * (lane & 1) + ((lane >> 4) & 1)) * 2 + (lane >> 5)) * 8,
                    __builtin_bit_cast(unsigned, (lane & 1) ? w1 : w0), tagb + T_PS);
        } else {
          GAS u16* o = (GAS u16*)p.out + (16 * S + 4 * wv) * D_ + 64 * cc + lane;
          o[0] = (u16)o0; o[D_] = (u16)(o0 >> 16); o[2 * D_] = (u16)o1; o[3 * D_] = (u16)(o1 >> 16);
        }
        if (late) {
          poll_flags(p.ws, fpoll, lane < 14, fhb + q + 2, 8);
          #pragma unroll
          for (int s = 0; s < 14; ++s) { Bh[G2_ORD[s]] = __builtin_amdgcn_raw_buffer_load_b128(rws, lane * 16, hn + rot.k(G2_ORD[s]) * 1024, SC1); GAP(); }
        }
        STAMP(24 + 24 * layer + q);
      }
      VMCLEAN();
      if (layer == 0 && lane == 0) st_flag(p.ws, f2off, tagb + T_F2 + 4);
      BAR();
    }
    STAMP(28 + 24 * layer);
    if (layer == 1) { STAMP_OUT(); break; }
    conv_in(1, []() {});
    conv_main(1, [](int) {});
    g1_begin(1);
  }
}

}

extern "C" void kernel_launch(void* const* d_in, const int* in_sizes, int n_in, void* d_out, int out_size, void* d_ws, size_t ws_size,
                              hipStream_t stream) {
  Params p;
  p.u = (const u16*)d_in[0]; p.lar = (const u16*)d_in[1]; p.aim = (const u16*)d_in[2]; p.cre = (const u16*)d_in[3];
  p.cim = (const u16*)d_in[4]; p.ldt = (const u16*)d_in[5]; p.dsk = (const u16*)d_in[6];
  p.wg = (const u16*)d_in[8]; p.wu = (const u16*)d_in[9]; p.wd = (const u16*)d_in[10];
  p.out = (u16*)d_out; p.ws = (unsigned char*)d_ws;
  (void)hipFuncSetAttribute((const void*)fused_kernel, hipFuncAttributeMaxDynamicSharedMemorySize, LDS_BYTES);
  hipLaunchKernelGGL(fused_kernel, dim3(256), dim3(256), LDS_BYTES, stream, p);
}
```

```cpp
#include <hip/hip_runtime.h>
#include <hip/hip_bf16.h>
#include <type_traits>
#include <utility>

namespace {

#define GAS __attribute__((address_space(1)))
#define LAS __attribute__((address_space(3)))
#define DI __device__ __forceinline__
#define AI __attribute__((always_inline))
#define GAP() __builtin_amdgcn_sched_barrier(0)
#define RLX_AGENT __ATOMIC_RELAXED, __HIP_MEMORY_SCOPE_AGENT
typedef float f32x16 __attribute__((ext_vector_type(16)));
typedef float f32x4 __attribute__((ext_vector_type(4)));
typedef float f32x2 __attribute__((ext_vector_type(2)));
typedef __bf16 bf16x2 __attribute__((ext_vector_type(2)));
typedef _Float16 f16x2 __attribute__((ext_vector_type(2)));
typedef short s16x4 __attribute__((ext_vector_type(4)));
typedef unsigned u32x4 __attribute__((ext_vector_type(4)));
typedef unsigned u32x2 __attribute__((ext_vector_type(2)));
typedef __amdgpu_buffer_rsrc_t rsrc_t;
typedef LAS unsigned char* lptr;
typedef unsigned short u16;

constexpr int L_ = 2048, D_ = 448, N_ = 256, F_ = 1792;
constexpr int SC1 = 16;
constexpr unsigned SPIN_CAP = 1u << 17;

constexpr unsigned WS_EPOCH = 0;
constexpr unsigned WS_FX = 4096;
constexpr unsigned WS_FH = WS_FX + 16384;
constexpr unsigned WS_F2 = WS_FH + 65536;
constexpr unsigned WS_ST = WS_F2 + 65536;
constexpr unsigned WS_PS = WS_ST + 65536;
constexpr unsigned WS_CS = WS_PS + 128 * 28 * 32 * 8;
constexpr unsigned WS_FW = WS_CS + 2 * F_ * 8;
constexpr unsigned WS_CTL_END = WS_FW + 32 * 64;
constexpr unsigned ACT_BYTES = L_ * D_ * 2;
constexpr unsigned WS_XT = 2u << 20;
constexpr unsigned WS_X1 = WS_XT + 2 * ACT_BYTES;
constexpr unsigned H_BYTES = L_ * F_ * 2;
constexpr unsigned WS_H = WS_X1 + 2 * ACT_BYTES;
constexpr unsigned WS_WD = WS_H + 2 * H_BYTES;
constexpr unsigned WS_DBG = WS_WD + D_ * F_ * 2;
constexpr unsigned WS_TOTAL = WS_DBG + (1u << 20);
static_assert(WS_CTL_END <= WS_XT, "control block");

constexpr unsigned T_FX = 0, T_FH = 16, T_F2 = 32, T_ST = 64, T_PS = 80, T_CS = 96, T_FW = 97;

constexpr unsigned LA_U = 128;
constexpr unsigned U_CH = 7600;
constexpr unsigned LA_KT = 15360;
constexpr unsigned KT_CP = 4160, KT_CH = 4 * KT_CP;
constexpr unsigned LA_Y = 48640;
constexpr unsigned XB_SZ = 28672;
constexpr unsigned PT_W = 5120, PT_W0 = 4096, PT_SZ = 4 * PT_W;
constexpr unsigned TB_W = 32 * 528;
constexpr unsigned LA_RED = 57856;
constexpr unsigned LA_MISC = 69952;
constexpr unsigned LA_DUMP = 70656;
constexpr unsigned LA_HS = 8192;
constexpr unsigned LA_KC = 73216;
constexpr unsigned LA_W = 81664;
constexpr unsigned W_PL = 40960;
constexpr unsigned LDS_BYTES = LA_W + 81920;
static_assert(LDS_BYTES <= 163840 && 4 * TB_W <= LA_MISC && LA_RED + 4 * 21 * 36 * 4 <= LA_MISC, "lds map");

struct Params {
  const u16 *u, *lar, *aim, *cre, *cim, *ldt, *dsk, *wg, *wu, *wd;
  u16* out;
  unsigned char* ws;
};

DI rsrc_t mk_rsrc(const void* p, unsigned bytes) { return __builtin_amdgcn_make_buffer_rsrc((void*)p, 0, (int)bytes, 0x00020000); }
DI float asf(unsigned x) { return __builtin_bit_cast(float, x); }
DI float bflo(unsigned x) { return __builtin_bit_cast(float, x << 16); }
DI float bfhi(unsigned x) { return __builtin_bit_cast(float, x & 0xffff0000u); }
DI unsigned pkbf(float lo, float hi) { f32x2 v = {lo, hi}; return __builtin_bit_cast(unsigned, __builtin_convertvector(v, bf16x2)); }
DI unsigned pkbf_s(float lo, float hi) { unsigned r; asm("v_cvt_pk_bf16_f32 %0, %1, %2" : "=v"(r) : "v"(lo), "v"(hi)); return r; }
DI unsigned pkh(float lo, float hi) { f32x2 v = {lo, hi}; return __builtin_bit_cast(unsigned, __builtin_convertvector(v, f16x2)); }
DI float rbf(float x) { return bflo(pkbf(x, x) & 0xffffu); }
DI u32x2 trr(lptr p) { return __builtin_bit_cast(u32x2, __builtin_amdgcn_ds_read_tr16_b64_v4i16((LAS s16x4*)p)); }
DI u32x4 cat(u32x2 a, u32x2 b) { return u32x4{a.x, a.y, b.x, b.y}; }
DI unsigned ld_flag(unsigned char* ws, unsigned off) { return __hip_atomic_load((GAS unsigned*)(ws + off), RLX_AGENT); }
DI void st_flag(unsigned char* ws, unsigned off, unsigned v) { __hip_atomic_store((GAS unsigned*)(ws + off), v, RLX_AGENT); }
DI void st_flag_if(rsrc_t rws, bool c, unsigned off, unsigned v) { __builtin_amdgcn_raw_buffer_store_b32(v, rws, c ? off : 0xfffffff0u, 0, SC1); }
DI void st_gran(unsigned char* ws, unsigned off, unsigned val, unsigned tag) {
  __hip_atomic_store((GAS unsigned long long*)(ws + off), ((unsigned long long)tag << 32) | val, RLX_AGENT);
}
#define BAR() asm volatile("s_waitcnt lgkmcnt(0)\n\ts_barrier" ::: "memory")
#define VMWAIT(n) asm volatile("s_waitcnt vmcnt(" #n ")" ::: "memory")
#define ORDER() __builtin_amdgcn_fence(__ATOMIC_ACQUIRE, "wavefront")

template <int CTRL> DI float dpp_add(float x) {
  return x + __builtin_bit_cast(float, __builtin_amdgcn_update_dpp(0, __builtin_bit_cast(int, x), CTRL, 0xf, 0xf, true));
}
template <int CTRL> DI unsigned dpp_umax(unsigned x) {
  unsigned y = (unsigned)__builtin_amdgcn_update_dpp(0, (int)x, CTRL, 0xf, 0xf, true); return x > y ? x : y;
}
constexpr int DPP_X1 = 0xB1, DPP_X2 = 0x4E, DPP_ROR4 = 0x124, DPP_ROR8 = 0x128;
DI float sw32_add(float a, float b) {
  auto r = __builtin_amdgcn_permlane32_swap(__builtin_bit_cast(unsigned, a), __builtin_bit_cast(unsigned, b), false, false);
  return asf(r[0]) + asf(r[1]);
}
DI float sw16_add(float a, float b) {
  auto r = __builtin_amdgcn_permlane16_swap(__builtin_bit_cast(unsigned, a), __builtin_bit_cast(unsigned, b), false, false);
  return asf(r[0]) + asf(r[1]);
}
DI float row_sum(float x) { return dpp_add<DPP_ROR8>(dpp_add<DPP_ROR4>(dpp_add<DPP_X2>(dpp_add<DPP_X1>(x)))); }
DI unsigned ln_pack(float s, float q) {
  const float m = s * (1.0f / D_);
  float v = q * (1.0f / D_) - m * m; v = v > 0.f ? v : 0.f;
  const float sd = __builtin_sqrtf(rbf(rbf(v) + 1e-5f));
  return pkbf(m, sd);
}

DI void mf32_first(f32x16& d, const u32x4& a, const u32x4& b) { asm volatile("v_mfma_f32_32x32x16_bf16 %0, %1, %2, 0" : "=&v"(d) : "a"(a), "v"(b)); }
DI void mf32_acc(f32x16& d, const u32x4& a, const u32x4& b) { asm volatile("v_mfma_f32_32x32x16_bf16 %0, %1, %2, %0" : "+v"(d) : "a"(a), "v"(b)); }
DI void mf32_vv(f32x16& d, const u32x4& a, const u32x4& b) { asm volatile("v_mfma_f32_32x32x16_bf16 %0, %1, %2, %0" : "+v"(d) : "v"(a), "v"(b)); }
DI void mf32_va(f32x16& d, const u32x4& a, const u32x4& b) { asm volatile("v_mfma_f32_32x32x16_bf16 %0, %1, %2, %0" : "+v"(d) : "v"(a), "a"(b)); }
DI void mh32_vv(f32x16& d, const u32x4& a, const u32x4& b) { asm volatile("v_mfma_f32_32x32x16_f16 %0, %1, %2, %0" : "+v"(d) : "v"(a), "v"(b)); }
DI void mf16_a0(f32x4& d, const u32x4& a, const u32x4& b) { asm volatile("v_mfma_f32_16x16x32_bf16 %0, %1, %2, 0" : "=&v"(d) : "a"(a), "v"(b)); }
DI void mf16_a(f32x4& d, const u32x4& a, const u32x4& b) { asm volatile("v_mfma_f32_16x16x32_bf16 %0, %1, %2, %0" : "+v"(d) : "a"(a), "v"(b)); }
DI void mf16_v(f32x4& d, const u32x4& a, const u32x4& b) { asm volatile("v_mfma_f32_16x16x32_bf16 %0, %1, %2, %0" : "+v"(d) : "v"(a), "v"(b)); }

template <int IMM, bool COH> DI void dma(unsigned voff, rsrc_t rs, unsigned soff) {
  if (COH) asm volatile("buffer_load_dwordx4 %0, %1, %2 offen offset:%3 sc1 lds" :: "v"(voff), "s"(rs), "s"(soff), "i"(IMM) : "memory");
  else asm volatile("buffer_load_dwordx4 %0, %1, %2 offen offset:%3 lds" :: "v"(voff), "s"(rs), "s"(soff), "i"(IMM) : "memory");
}
DI void set_m0(unsigned v) { asm volatile("s_mov_b32 m0, %0\n\ts_nop 0" :: "s"(v) : "memory"); }

DI int fresh(int x) { asm volatile("" : "+v"(x)); return x; }
#define STAMP(i) do {} while (0)
#define STAMP_DECL do {} while (0)
#define STAMP_OUT() do {} while (0)

#define VMCLEAN() do { VMWAIT(0); __builtin_amdgcn_s_waitcnt(0x0F70); } while (0)
#define RELOAD() asm volatile("" ::: "memory")
DI void poll_flags(unsigned char* ws, unsigned off, bool active, unsigned want, unsigned win) {
  if (active) {
    #pragma unroll 1
    for (unsigned n = 0; n < SPIN_CAP; ++n)
      if ((ld_flag(ws, off) - want) < win) break;
  }
  VMCLEAN();
}

DI u32x4 wd_load(rsrc_t rwd, unsigned vo, int kstep, int j) { return __builtin_amdgcn_raw_buffer_load_b128(rwd, vo + j * 896, kstep * 32 * 896, 0); }
DI void wd_conv(const u32x4* r, u32x4* f) {
  unsigned lo[4][2], hi[4][2];
  #pragma unroll
  for (int j = 0; j < 4; ++j)
    #pragma unroll
    for (int i = 0; i < 2; ++i) {
      const unsigned a = r[j][i], b = r[j][2 + i];
      lo[j][i] = (unsigned)__builtin_amdgcn_update_dpp((int)a, (int)b, DPP_ROR8, 0xf, 0xc, false);
      hi[j][i] = (unsigned)__builtin_amdgcn_update_dpp((int)b, (int)a, DPP_ROR8, 0xf, 0x3, false);
    }
  #pragma unroll
  for (int t = 0; t < 4; ++t) {
    const unsigned sel = (t & 1) ? 0x07060302u : 0x05040100u;
    f[t] = u32x4{__builtin_amdgcn_perm(lo[1][t >> 1], lo[0][t >> 1], sel), __builtin_amdgcn_perm(lo[3][t >> 1], lo[2][t >> 1], sel),
                 __builtin_amdgcn_perm(hi[1][t >> 1], hi[0][t >> 1], sel), __builtin_amdgcn_perm(hi[3][t >> 1], hi[2][t >> 1], sel)};
  }
  asm volatile("" : "+v"(f[0]), "+v"(f[1]), "+v"(f[2]), "+v"(f[3]));
  GAP();
}
template <int B, class F, int... I> DI void sfor_(F&& f, std::integer_sequence<int, I...>) { (f(std::integral_constant<int, B + I>{}), ...); }
template <int B, int E, class F> DI void sfor(F&& f) { sfor_<B>(f, std::make_integer_sequence<int, E - B>{}); }
DI void vmwait(int n) {
  switch (n) {
    case 0: VMWAIT(0); break;
    case 1: VMWAIT(1); break;
    case 2: VMWAIT(2); break;
    case 3: VMWAIT(3); break;
    case 4: VMWAIT(4); break;
    case 5: VMWAIT(5); break;
    case 6: VMWAIT(6); break;
    case 7: VMWAIT(7); break;
    case 8: VMWAIT(8); break;
    case 9: VMWAIT(9); break;
    case 10: VMWAIT(10); break;
    case 11: VMWAIT(11); break;
    case 12: VMWAIT(12); break;
    case 13: VMWAIT(13); break;
    case 14: VMWAIT(14); break;
    case 15: VMWAIT(15); break;
    case 16: VMWAIT(16); break;
    case 17: VMWAIT(17); break;
    case 18: VMWAIT(18); break;
    case 19: VMWAIT(19); break;
    case 20: VMWAIT(20); break;
    case 21: VMWAIT(21); break;
    case 22: VMWAIT(22); break;
    case 23: VMWAIT(23); break;
    case 24: VMWAIT(24); break;
    case 25: VMWAIT(25); break;
    case 26: VMWAIT(26); break;
    case 27: VMWAIT(27); break;
    case 28: VMWAIT(28); break;
    case 29: VMWAIT(29); break;
    case 30: VMWAIT(30); break;
    case 31: VMWAIT(31); break;
    case 32: VMWAIT(32); break;
    case 33: VMWAIT(33); break;
    case 34: VMWAIT(34); break;
    case 35: VMWAIT(35); break;
    case 36: VMWAIT(36); break;
    case 37: VMWAIT(37); break;
    case 38: VMWAIT(38); break;
    case 39: VMWAIT(39); break;
    case 40: VMWAIT(40); break;
    default: VMWAIT(0); break;
  }
}

DI void helper_main(const Params& p, lptr lds, unsigned tagb, int xg, int hp, int lane, int wv) {
  const rsrc_t rws = mk_rsrc(p.ws, WS_TOTAL);
  STAMP_DECL;
  STAMP(0);
  {
    const int hid = 4 * xg + hp;
    const rsrc_t rwd = mk_rsrc(p.wd, D_ * F_ * 2);
    const unsigned vo = (16 * (lane >> 5) + 4 * ((lane >> 4) & 1) + 8 * ((lane >> 3) & 1)) * 896 + (lane & 7) * 16;
    u32x4 raw[16];
    #pragma unroll
    for (int i = 0; i < 4; ++i) {
      const int n = min(hid + 32 * (wv + 4 * i), 391), ct = n / 56;
      #pragma unroll
      for (int j = 0; j < 4; ++j) raw[4 * i + j] = wd_load(rwd, vo + ct * 128, n - 56 * ct, j);
    }
    #pragma unroll
    for (int i = 0; i < 4; ++i) {
      const int n = hid + 32 * (wv + 4 * i);
      u32x4 f[4]; wd_conv(&raw[4 * i], f);
      if (n < 392) {
        #pragma unroll
        for (int t = 0; t < 4; ++t) __builtin_amdgcn_raw_buffer_store_b128(f[t], rws, lane * 16 + t * 1024, WS_WD + n * 4096, SC1);
      }
    }
    VMCLEAN(); BAR();
    if (wv == 0 && lane == 0) st_flag(p.ws, WS_FW + hid * 64, tagb + T_FW);
    STAMP(1);
  }
  {
    const int hid = 4 * xg + hp, tid = 64 * wv + lane, co = tid % 14, rg = tid / 14;
    const rsrc_t rw = mk_rsrc(hid < 16 ? p.wg : p.wu, D_ * F_ * 2);
    const unsigned vo = rg < 16 ? rg * 28 * F_ * 2 + ((hid & 15) * 112 + co * 8) * 2 : 0xfffffff0u;
    f32x2 a[4] = {};
    #pragma unroll 1
    for (int r0 = 0; r0 < 28; r0 += 14) {
      u32x4 v[14];
      #pragma unroll
      for (int r = 0; r < 14; ++r) v[r] = __builtin_amdgcn_raw_buffer_load_b128(rw, vo, (r0 + r) * F_ * 2, 0);
      #pragma unroll
      for (int r = 0; r < 14; ++r)
        #pragma unroll
        for (int d = 0; d < 4; ++d) a[d] += f32x2{bflo(v[r][d]), bfhi(v[r][d])};
    }
    if (rg < 16) {
      *(LAS f32x4*)(lds + rg * 448 + co * 32) = f32x4{a[0].x, a[0].y, a[1].x, a[1].y};
      *(LAS f32x4*)(lds + rg * 448 + co * 32 + 16) = f32x4{a[2].x, a[2].y, a[3].x, a[3].y};
    }
    BAR();
    if (tid < 112) {
      float t = 0.f;
      #pragma unroll
      for (int r = 0; r < 16; ++r) t += *(LAS float*)(lds + r * 448 + tid * 4);
      st_gran(p.ws, WS_CS + (hid * 112 + tid) * 8, __builtin_bit_cast(unsigned, t), tagb + T_CS);
    }
  }
  STAMP(2);
  #pragma unroll 1
  for (int layer = 0; layer < 2; ++layer) {
    const unsigned hid = 4 * xg + hp;
    #pragma unroll 1
    for (int half = 0; half < 2; ++half) {
      poll_flags(p.ws, WS_FX + (56 * wv + lane) * 64, lane < 56, tagb + T_FX + 2 * layer + half + 1, 4);
      STAMP(3 + 8 * layer + 2 * half);
      u32x4 v[7];
      #pragma unroll
      for (int i = 0; i < 7; ++i) v[i] = __builtin_amdgcn_raw_buffer_load_b128(rws, lane * 16 + i * 1024, WS_X1 + layer * ACT_BYTES + (32 * half + hid) * XB_SZ + wv * 7168, SC1);
      f32x2 s[4] = {}, sq[4] = {};
      #pragma unroll
      for (int i = 0; i < 7; ++i)
        #pragma unroll
        for (int d = 0; d < 4; ++d) { f32x2 x = {bflo(v[i][d]), bfhi(v[i][d])}; s[d] += x; sq[d] = __builtin_elementwise_fma(x, x, sq[d]); }
      float w[4];
      #pragma unroll
      for (int d = 0; d < 4; ++d) w[d] = dpp_add<DPP_ROR8>(dpp_add<DPP_ROR4>(sw16_add(sw32_add(s[d].x, sq[d].x), sw32_add(s[d].y, sq[d].y))));
      if ((lane & 12) == 0) {
        LAS float* o = (LAS float*)(lds + LA_HS + half * 1024 + wv * 256 + (lane >> 5) * 128) + 8 * (lane & 3) + ((lane >> 4) & 1);
        #pragma unroll
        for (int d = 0; d < 4; ++d) o[2 * d] = w[d];
      }
      BAR();
      if (wv == 0 && lane < 32) {
        LAS float* o = (LAS float*)(lds + LA_HS + half * 1024) + lane;
        const float a = (o[0] + o[64]) + (o[128] + o[192]), b = (o[32] + o[96]) + (o[160] + o[224]);
        st_gran(p.ws, WS_ST + (2 * layer + 1) * 16384 + ((32 * half + hid) * 32 + lane) * 8, ln_pack(a, b), tagb + T_ST + 2 * layer + 1);
      }
      STAMP(4 + 8 * layer + 2 * half);
    }
    if (layer == 0) {
      const unsigned S = 32 * wv + hid;
      u32x4 g[7];
      #pragma unroll 1
      for (unsigned n = 0; n < SPIN_CAP; ++n) {
        RELOAD();
        bool ok = true;
        #pragma unroll
        for (int i = 0; i < 7; ++i) g[i] = __builtin_amdgcn_raw_buffer_load_b128(rws, lane * 16 + i * 1024, WS_PS + S * 7168, SC1);
        #pragma unroll
        for (int i = 0; i < 7; ++i) ok = ok && g[i].y == tagb + T_PS && g[i].w == tagb + T_PS;
        if (ok) break;
      }
      STAMP(7);
      float a = 0.f, b = 0.f;
      #pragma unroll
      for (int i = 0; i < 7; ++i) { a += asf(g[i].x); b += asf(g[i].z); }
      a = sw32_add(a, b);
      a = sw16_add(a, a);
      b = asf(__builtin_amdgcn_permlane32_swap(__builtin_bit_cast(unsigned, a), __builtin_bit_cast(unsigned, a), false, false)[1]);
      if (lane < 16) st_gran(p.ws, WS_ST + 2 * 16384 + (S * 16 + lane) * 8, ln_pack(a, b), tagb + T_ST + 2);
      STAMP(8);
    }
    BAR();
  }
  STAMP_OUT();
}

constexpr int G2_ORD[14] = {0, 1, 9, 2, 10, 3, 11, 4, 12, 5, 13, 6, 7, 8};
struct Wts { u32x4 W1[56]; u32x4 WdA[8]; u32x4 WdV[28]; u32x4 WS[2]; };

struct Rot { int a, b, st; DI int k(int o) const { return (o < 7 ? a : b) + (o % 7) * st; } };
DI void w1_piece(rsrc_t rw, unsigned vo, const unsigned (&gb)[4], unsigned m0w, int k0, int i) {
  const int slot = k0 + (i >> 1);
  asm volatile("s_mov_b32 m0, %0" :: "s"(m0w + i * 2048) : "memory");
  const unsigned so = gb[slot / 7] + ((slot % 7) * 16 + (i & 1) * 8) * F_ * 2;
  asm volatile("s_nop 0\n\tbuffer_load_dwordx4 %0, %1, %2 offen lds" :: "v"(vo), "s"(rw), "s"(so) : "memory");
}
template <int K0, int NK> DI void w1_read(Wts& w, lptr lds, unsigned rb) {
  #pragma unroll
  for (int s = 0; s < NK; ++s)
    #pragma unroll
    for (int t = 0; t < 2; ++t) {
      lptr a = lds + (rb ^ (t * 32)) + s * 4096;
      w.W1[2 * (K0 + s) + t] = cat(trr(a), trr(a + 1024));
    }
}
template <int K0, int NK> DI void w1_pin(Wts& w) {
  #pragma unroll
  for (int s = 2 * K0; s < 2 * (K0 + NK); ++s) asm volatile("" : "+a"(w.W1[s]));
}

DI void kt_build(lptr lds, int tid) {
  #pragma unroll
  for (int ch = 0; ch < 2; ++ch) {
    const u32x4 a = *(LAS u32x4*)(lds + LA_KC + ch * 4160 + tid * 16);
    const u32x2 b = *(LAS u32x2*)(lds + LA_KC + ch * 4160 + tid * 16 + 16);
    const unsigned e0 = __builtin_amdgcn_alignbyte(a.y, a.x, 2), e1 = __builtin_amdgcn_alignbyte(a.z, a.y, 2),
                   e2 = __builtin_amdgcn_alignbyte(a.w, a.z, 2), e3 = __builtin_amdgcn_alignbyte(b.x, a.w, 2),
                   e4 = __builtin_amdgcn_alignbyte(b.y, b.x, 2);
    lptr o = lds + LA_KT + ch * KT_CH + tid * 16;
    *(LAS u32x4*)(o) = a;
    *(LAS u32x4*)(o + KT_CP) = u32x4{e0, e1, e2, e3};
    *(LAS u32x4*)(o + 2 * KT_CP) = u32x4{a.y, a.z, a.w, b.x};
    *(LAS u32x4*)(o + 3 * KT_CP) = u32x4{e1, e2, e3, e4};
  }
  const u32x4 z = {0, 0, 0, 0};
  if (tid < 32) *(LAS u32x4*)(lds + LA_KT + (tid >> 2) * KT_CP + 4096 + (tid & 3) * 16) = z;
  if (tid < 248) { const int ch = tid >= 124, i = tid - 124 * ch; *(LAS u32x4*)(lds + LA_U + ch * U_CH + (i >> 2) * 80 + (i & 3) * 16) = z; }
}

DI u32x4 ld2x64(lptr p) { return cat(*(volatile LAS u32x2*)(p), *(volatile LAS u32x2*)(p + 8)); }
template <bool FIRST, class F> DI void conv_pass(f32x16& acc, lptr pa, lptr pb, F&& ride) {
  u32x4 A[3], B[3];
  auto off_a = [](int n) AI { return 32 * (n & 1) - 64 * (n >> 1) + 960; };
  auto off_b = [](int n) AI { return 32 * (n & 1) - 80 * (n >> 1) + 1200; };
  #pragma unroll
  for (int n = 0; n < 2; ++n) { A[n] = ld2x64(pa + off_a(n)); B[n] = *(LAS u32x4*)(pb + off_b(n)); }
  GAP();
  sfor<0, 32>([&](auto nc) AI {
    constexpr int n = decltype(nc)::value;
    if (FIRST && n == 0) asm volatile("v_mfma_f32_32x32x16_f16 %0, %1, %2, 0" : "=&v"(acc) : "v"(A[0]), "v"(B[0])); else mh32_vv(acc, A[n % 3], B[n % 3]);
    GAP();
    if (n + 2 < 32) { A[(n + 2) % 3] = ld2x64(pa + off_a(n + 2)); B[(n + 2) % 3] = *(LAS u32x4*)(pb + off_b(n + 2)); }
    GAP();
    ride(n);
    GAP();
  });
}

template <int Q, class F>
DI void g1_step(const Wts& w, lptr lds, f32x16 (&cur)[2], f32x16 (&prev)[2], u32x4 (&B)[3], unsigned rd_cur, unsigned rd_next,
                float c1, float sd, unsigned negm, rsrc_t rws, unsigned hvo, unsigned hso,
                unsigned xvo, unsigned m0base, unsigned xso,
                u32x2& stn, unsigned stvo, unsigned stso, unsigned char* ws, unsigned fhoff, unsigned fhval, int lane, int& nh, unsigned& pfx, unsigned fxo, F&& ride) {
  constexpr bool EPI = Q > 0, MMA = Q < 4, DMA = Q == 1 || Q == 2;
  float e[16]; unsigned pk[8];
  auto gidx = [](int i) AI { return ((i >> 2) & 1) * 8 + (i & 3); };
  constexpr int E0 = Q == 3 ? 3 : 8;
  int nx = 0, ns = 0;
  if (EPI && !MMA) { u32x4 bm = {negm, negm, negm, negm}; asm volatile("s_nop 1" : "+v"(bm)); mf32_vv(prev[0], w.WS[0], bm); mf32_vv(prev[1], w.WS[1], bm);
    asm volatile("s_nop 11" : "+v"(prev[0]), "+v"(prev[1])); }
  sfor<MMA ? 0 : E0, 28>([&](auto sc) AI {
    constexpr int s = decltype(sc)::value;
    if (MMA) {
      if (s == 0) mf32_first(cur[0], w.W1[0], B[0]); else mf32_acc(cur[0], w.W1[2 * s], B[s % 3]);
      GAP();
      const int sp = s + 2; lptr a = lds + (sp < 28 ? rd_cur + sp * 1024 : rd_next + (sp - 28) * 1024);
      if (sp < 28 || Q < 3) B[sp % 3] = cat(trr(a), trr(a + 256));
      GAP();
      if (EPI && s == 0) { u32x4 bm = {negm, negm, negm, negm}; asm volatile("s_nop 1" : "+v"(bm)); mf32_vv(prev[0], w.WS[0], bm); mf32_vv(prev[1], w.WS[1], bm); }
    }
    if (EPI) {
      constexpr int i = s - E0;
      if constexpr (i - 1 >= 0 && i - 1 < 16) e[i - 1] = __builtin_amdgcn_exp2f(e[i - 1]);
      if constexpr (i >= 0 && i < 16) e[i] = prev[i >> 3][gidx(i)] * c1;
      if constexpr (i - 2 >= 0 && i - 2 < 16) e[i - 2] = __builtin_fmaf(e[i - 2], sd, sd);
    }
    GAP();
    if (MMA) {
      if (s == 0) mf32_first(cur[1], w.W1[1], B[0]); else mf32_acc(cur[1], w.W1[2 * s + 1], B[s % 3]);
      GAP();
    }
    if (EPI) {
      constexpr int i = s - E0;
      if constexpr (i - 2 >= 0 && i - 2 < 16) e[i - 2] = __builtin_amdgcn_rcpf(e[i - 2]);
      if constexpr (i - 3 >= 0 && i - 3 < 16) e[i - 3] = prev[(i - 3) >> 3][gidx(i - 3) + 4] * e[i - 3];
      constexpr int j = i - 4;
      if constexpr (j >= 0 && j < 16 && (j & 1)) pk[j >> 1] = pkbf_s(e[j - 1], e[j]);
      if (j == 8) { __builtin_amdgcn_raw_buffer_store_b128(u32x4{pk[0], pk[1], pk[2], pk[3]}, rws, hvo, hso, SC1); ++nx; ++nh; ++ns; }
      if (j == 15) { GAP(); __builtin_amdgcn_raw_buffer_store_b128(u32x4{pk[4], pk[5], pk[6], pk[7]}, rws, hvo + 512, hso, SC1); nh = 0; ++nx; ++ns; }
      if (Q == 4 && s == 12) { vmwait(nh); st_flag_if(rws, lane == 0, fhoff, fhval); ++nh; }
    }
    GAP();
    constexpr int D0 = Q == 1 ? 6 : 0;
    if (Q == 1 && s == 0) { asm volatile("buffer_load_dword %0, %1, %2, 0 offen sc1" : "=v"(pfx) : "v"(fxo), "s"(rws) : "memory"); ++nh; }
    if (Q == 1 && s == D0) {
      VMWAIT(0); asm volatile("" : "+v"(pfx));
      if (!__all(lane >= 56 || (pfx - fhval) < 4u)) poll_flags(ws, fxo, lane < 56, fhval, 4);
    }
    if (DMA && s >= D0 && s < D0 + 7) {
      if (s == D0) { asm volatile("s_barrier" ::: "memory");
                     set_m0(m0base); dma<0, true>(xvo, rws, xso); }
      if (s == D0 + 1) dma<1024, true>(xvo, rws, xso);
      if (s == D0 + 2) dma<2048, true>(xvo, rws, xso);
      if (s == D0 + 3) dma<3072, true>(xvo, rws, xso);
      if (s == D0 + 4) { set_m0(m0base + 4096); dma<0, true>(xvo, rws, xso + 4096); }
      if (s == D0 + 5) dma<1024, true>(xvo, rws, xso + 4096);
      if (s == D0 + 6) dma<2048, true>(xvo, rws, xso + 4096);
      ++nh; ++ns; nx = 0;
    }
    if (MMA) {
      if (s == 13 && Q >= 2) { vmwait(nh); st_flag_if(rws, lane == 0, fhoff, fhval); ++nx; ++nh; ++ns; }
      if (s == (Q == 0 ? 16 : 12)) { asm volatile("buffer_load_dwordx2 %0, %1, %2, %3 offen sc1" : "=v"(stn) : "v"(stvo), "s"(rws), "s"(stso) : "memory"); ++nx; ++nh; ns = 0;
      }
      if (s == 25 && Q < 3) { vmwait(nx); asm volatile("s_barrier" ::: "memory"); }
    }
    GAP();
    { const int k = ride(s); nx += k; nh += k; ns += k; }
    GAP();
  });
  if (MMA) { vmwait(ns); asm volatile("" : "+v"(stn)); }
}

DI void stats_take(u32x2& g, rsrc_t rws, unsigned vo, unsigned so, unsigned tag, float& c1, float& sd, unsigned& negm) {
  #pragma unroll 1
  for (unsigned n = 0; n < SPIN_CAP && g.y != tag; ++n) { RELOAD(); g = __builtin_amdgcn_raw_buffer_load_b64(rws, vo, so, SC1); }
  sd = bfhi(g.x);
  c1 = -1.4426950408889634f * __builtin_amdgcn_rcpf(sd);
  const unsigned nm = (g.x & 0xffffu) ^ 0x8000u;
  negm = nm | (nm << 16);
}

__global__ void __launch_bounds__(256, 1) fused_kernel(Params p) {
  extern __shared__ __attribute__((aligned(16))) unsigned char smem[];
  const lptr lds = (lptr)smem;
  const int tid = threadIdx.x, lane = tid & 63, wv = __builtin_amdgcn_readfirstlane(tid >> 6);
  const int b = blockIdx.x, xg = b & 7, idx = b >> 3;
  const bool worker = idx < 28;
  STAMP_DECL;
  STAMP(0);
  const rsrc_t rws = mk_rsrc(p.ws, WS_TOTAL);

  const unsigned epoch = ld_flag(p.ws, WS_EPOCH);
  const rsrc_t ru = mk_rsrc(p.u, ACT_BYTES);
  u32x4 xr[2];
  #pragma unroll
  for (int i = 0; i < 2; ++i) xr[i] = __builtin_amdgcn_raw_buffer_load_b128(ru, lane < 56 ? lane * 16 : 0xfffffff0u, (8 * b + 2 * wv + i) * 896, 0);
  const int c0 = worker ? 56 * xg + 2 * idx : 0;
  const int kch = c0 + (wv >> 1);
  unsigned pr[4];
  {
    const int o = (kch * N_ + (wv & 1) * 128) / 2 + lane;
    pr[0] = ((const unsigned*)p.lar)[o]; pr[1] = ((const unsigned*)p.aim)[o]; pr[2] = ((const unsigned*)p.cre)[o]; pr[3] = ((const unsigned*)p.cim)[o];
  }
  const unsigned ldt2 = *(const unsigned*)(p.ldt + c0), dsk2 = *(const unsigned*)(p.dsk + c0);
  const int sid = idx & 3;
  const int gam = 4 * (xg & 3) + sid, js = 7 * (xg >> 2) + (idx >> 2);
  const int s2 = 4 * xg + sid, cc = idx >> 2;
  const Rot rot = {14 * wv + ((sid & 1) ? 7 : 0) + ((sid & 2) ? 6 : 0), 14 * wv + ((sid & 1) ? 0 : 7) + ((sid & 2) ? 6 : 0), (sid & 2) ? -1 : 1};
  {
    unsigned long long pc; unsigned len;
    asm volatile("s_getpc_b64 %0\n.Lfused_code_here:\n\ts_mov_b32 %1, .Lfused_code_end-.Lfused_code_here" : "=s"(pc), "=s"(len));
    const rsrc_t rc = mk_rsrc((const void*)(pc & ~15ull), len & ~15u);
    set_m0(LA_DUMP);
    #pragma unroll
    for (int i = 0; i < 2; ++i) dma<0, false>(lane * 16, rc, (i * 32 + (idx & 7) * 4 + wv) * 1024);
  }
  const rsrc_t rw1 = mk_rsrc(wv < 2 ? p.wg : p.wu, D_ * F_ * 2);
  const unsigned w1vo = (4 * (wv & 1) + (lane >> 4)) * F_ * 2 + (((lane & 15) ^ ((2 * (lane >> 4) + (wv >> 1)) << 1)) * 16);
  const unsigned w1m0 = LA_W + (wv >> 1) * W_PL + (wv & 1) * 1024;
  unsigned w1gb[4];
  #pragma unroll
  for (int g = 0; g < 4; ++g) w1gb[g] = ((g + sid) & 3) * 112 * F_ * 2 + js * 256;
  unsigned kidx[8];
  #pragma unroll
  for (int j = 0; j < 8; ++j) {
    const int l = 2047 - 8 * tid - j;
    const unsigned pos = (unsigned)rbf((float)l), hb = pos >> 8;
    const unsigned sh = hb >= 4 ? 3u : (hb >= 2 ? 2u : hb);
    const unsigned base = (unsigned)((0x14ull << 40 | 0x13ull << 35 | 0x12ull << 30 | 0x11ull << 25 | 0x10ull << 20 | 0xeull << 15 | 0xcull << 10 | 0x8ull << 5) >> (5 * hb)) & 31u;
    kidx[j] = ((base + ((pos & 7u) >> sh)) * 36 + ((pos & 255u) >> 3)) * 4;
    asm volatile("" : "+v"(kidx[j]));
  }
  GAP();
  const unsigned ep = __builtin_amdgcn_readfirstlane(epoch), tagb = (ep + 1u) * 0x9E3779B1u;

  unsigned st1;
  {
    float s[2], q[2];
    asm volatile("" : "+v"(xr[0]), "+v"(xr[1]), "+v"(pr[0]), "+v"(pr[1]), "+v"(pr[2]), "+v"(pr[3]));
    if (worker) {
      #pragma unroll
      for (int n = 0; n < 4; ++n) w1_piece(rw1, w1vo, w1gb, w1m0, 0, n);
    }
    #pragma unroll
    for (int i = 0; i < 2; ++i) {
      f32x2 a = {0.f, 0.f}, c = {0.f, 0.f};
      #pragma unroll
      for (int d = 0; d < 4; ++d) { f32x2 x = {bflo(xr[i][d]), bfhi(xr[i][d])}; a += x; c = __builtin_elementwise_fma(x, x, c); }
      s[i] = a.x + a.y; q[i] = c.x + c.y;
      if (lane < 56) *(LAS u32x4*)(lds + LA_KC + (2 * wv + i) * 896 + lane * 16) = xr[i];
    }
    float r = row_sum(sw16_add(sw32_add(s[0], q[0]), sw32_add(s[1], q[1])));
    const float o = asf(__builtin_amdgcn_permlane32_swap(__builtin_bit_cast(unsigned, r), __builtin_bit_cast(unsigned, r), false, false)[1]);
    st1 = ln_pack(r, o);
    BAR();
    #pragma unroll
    for (int i = 0; i < 2; ++i) {
      const int g = 4 * i + (lane >> 4), cg = wv * 7 + (g < 7 ? g : 0);
      const lptr a = lds + LA_KC + ((lane >> 2) & 3) * 896 + cg * 32 + (lane & 3) * 8;
      __builtin_amdgcn_raw_buffer_store_b128(cat(trr(a), trr(a + 4 * 896)), rws, g < 7 ? (cg * 16 + (lane & 15)) * 16 : 0xfffffff0u, WS_XT + b * D_ * 16, SC1);
    }
  }
  const unsigned st1off = WS_ST + (8 * b + 2 * wv + (lane >> 4)) * 8;
  if (!worker) {
    VMWAIT(0); BAR();
    if (lane == 0 || lane == 16) st_gran(p.ws, st1off, st1, tagb + T_ST);
    helper_main(p, lds, tagb, xg, idx - 28, lane, wv);
    return;
  }
  STAMP(1);

  Wts w;
  const unsigned w1rb = LA_W + ((lane >> 1) & 1) * W_PL + (8 * (lane >> 5) + ((lane >> 2) & 3)) * 256 +
                        (((4 * wv + ((lane >> 4) & 1)) ^ ((2 * ((lane >> 2) & 3) + ((lane >> 1) & 1)) << 1)) * 16) + (lane & 1) * 8;

  const float dk = (tid & 1) ? bfhi(dsk2) : bflo(dsk2);
  u32x4 gr[2][4], xa[2], uh[2]; unsigned f2w; float base[2][8];
  auto fetch = [&](const int layer, const int half) AI {
    const int t = fresh(threadIdx.x);
    #pragma unroll
    for (int i = 0; i < 4; ++i) gr[half][i] = __builtin_amdgcn_raw_buffer_load_b128(rws, (t >> 1) * 64 + i * 16, WS_ST + 2 * layer * 16384 + half * 8192, SC1);
    GAP();
  };
  auto valid = [&](const int layer, const int half) AI {
    const unsigned tag = tagb + T_ST + 2 * layer;
    bool ok = true;
    #pragma unroll
    for (int i = 0; i < 4; ++i) ok = ok && gr[half][i].y == tag && gr[half][i].w == tag;
    return ok;
  };
  auto settle = [&](const int layer, const int half) AI {
    if (!__all(valid(layer, half))) {
      #pragma unroll 1
      for (unsigned n = 0; n < SPIN_CAP; ++n) { RELOAD(); fetch(layer, half); if (valid(layer, half)) break; }
      VMCLEAN();
    }
  };
  auto f2_off = [&]() AI { return WS_F2 + ((((fresh(threadIdx.x) >> 2) & 31) * 7 + (c0 >> 6)) * 4 + ((c0 >> 4) & 3)) * 64; };
  auto f2_load = [&]() AI { f2w = __builtin_amdgcn_raw_buffer_load_b32(rws, f2_off(), 0, SC1); GAP(); };
  auto f2_ok = [&](const int half) AI { asm volatile("" : "+v"(f2w) :: "memory"); return (bool)__all((f2w - (tagb + T_F2 + 2 * half + 2)) < 3u); };
  auto f2_wait = [&](const int half) AI { poll_flags(p.ws, f2_off(), true, tagb + T_F2 + 2 * half + 2, 3); };
  auto xload = [&](const int layer, const int half, rsrc_t r) AI {
    const int t = fresh(threadIdx.x);
    const unsigned vo = layer ? ((t >> 2) * D_ + c0 + (t & 1)) * 32 + (t & 2) * 8 : ((t >> 1) * D_ + c0 + (t & 1)) * 16;
    xa[half] = __builtin_amdgcn_raw_buffer_load_b128(r, vo, WS_XT + layer * ACT_BYTES + half * (ACT_BYTES / 2), SC1);
    GAP();
  };
  auto normalise = [&](const int half) AI {
    float u[8];
    #pragma unroll
    for (int j = 0; j < 8; ++j) {
      const unsigned v = (j & 1) ? gr[half][j >> 1].z : gr[half][j >> 1].x, wd = xa[half][j >> 1];
      const float x = (j & 1) ? bfhi(wd) : bflo(wd);
      u[j] = (x - bflo(v)) * __builtin_amdgcn_rcpf(bfhi(v));
      base[half][j] = __builtin_fmaf(u[j], dk, x);
    }
    uh[half] = u32x4{pkh(u[0], u[1]), pkh(u[2], u[3]), pkh(u[4], u[5]), pkh(u[6], u[7])};
  };
  auto u_write = [&](const int half) AI {
    const int t = fresh(threadIdx.x);
    *(LAS u32x4*)(lds + LA_U + (t & 1) * U_CH + ((t >> 3) + 32 * half + 31) * 80 + ((t >> 1) & 3) * 16) = uh[half];
  };

  const float dt0 = rbf(expf(bflo(ldt2))), dt1 = rbf(expf(bfhi(ldt2)));
  float ysc;
  {
    const float dt = (wv >> 1) ? dt1 : dt0;
    unsigned mx = max(max(pr[2] & 0x7fffu, pr[3] & 0x7fffu), max((pr[2] >> 16) & 0x7fffu, (pr[3] >> 16) & 0x7fffu));
    mx = dpp_umax<DPP_ROR8>(dpp_umax<DPP_ROR4>(dpp_umax<DPP_X2>(dpp_umax<DPP_X1>(mx))));
    unsigned eb = max(max(__builtin_amdgcn_readlane(mx, 0), __builtin_amdgcn_readlane(mx, 16)), max(__builtin_amdgcn_readlane(mx, 32), __builtin_amdgcn_readlane(mx, 48))) >> 7;
    eb = min(max(eb, 1u), 253u);
    const float cs = __builtin_bit_cast(float, (254u - eb) << 23);
    const lptr tb = lds + wv * TB_W;
    STAMP(2);
    auto ride = [&](int n) AI { w1_piece(rw1, w1vo, w1gb, w1m0, 0, n); GAP(); };
    f32x2 z1[2], z1r[2], zb[2], zbr[2], z8[2], zs[2], zsr[2], v[2][21];
    #pragma unroll
    for (int m = 0; m < 2; ++m) {
      auto part = [&](unsigned x) AI { return m ? bfhi(x) : bflo(x); };
      const float lr = -rbf(expf(part(pr[0]))) * dt * 1.4426950408889634f;
      const float li = part(pr[1]) * dt * 0.15915494309189535f;
      auto zpow = [&](float pw) AI { const float a = __builtin_amdgcn_exp2f(lr * pw); float t = li * pw; t -= __builtin_rintf(t);
                                     return f32x2{a * __builtin_amdgcn_cosf(t), a * __builtin_amdgcn_sinf(t)}; };
      const f32x2 y1 = zpow(1.f), yb = zpow(256.f);
      z8[m] = zpow(8.f); zs[m] = zpow(16.f); zsr[m] = f32x2{-zs[m].y, zs[m].x};
      z1[m] = f32x2{y1.x, -y1.y}; z1r[m] = f32x2{y1.y, y1.x};
      zb[m] = f32x2{yb.x, -yb.y}; zbr[m] = f32x2{yb.y, yb.x};
      v[m][0] = f32x2{part(pr[2]) * cs, -part(pr[3]) * cs};
    }
    GAP();
    volatile LAS u32x2* o = (volatile LAS u32x2*)(tb + lane * 8);
    #pragma unroll
    for (int r = 0; r < 21; ++r) {
      const int c = r + 1, sc = c < 8 ? c - 1 : c < 12 ? 2 * (c - 8) : c == 12 ? 8 : c == 13 ? 10 : c < 17 ? c - 2 : c - 1;
      f32x2 t0, t1;
      if (c < 21) { t0 = v[0][sc].xx * (c < 8 ? z1[0] : zb[0]); GAP(); t1 = v[1][sc].xx * (c < 8 ? z1[1] : zb[1]); GAP(); }
      const unsigned p0 = pkh(v[0][r].x, v[0][r].y); GAP();
      const unsigned p1 = pkh(v[1][r].x, v[1][r].y); GAP();
      if (c < 21) { v[0][c] = __builtin_elementwise_fma(v[0][sc].yy, c < 8 ? z1r[0] : zbr[0], t0); GAP();
                    v[1][c] = __builtin_elementwise_fma(v[1][sc].yy, c < 8 ? z1r[1] : zbr[1], t1); GAP(); }
      o[r * 66] = u32x2{p0, p1}; GAP();
      if (r % 2 == 0) ride(4 + r / 2);
      if (r == 7) {
        asm volatile("s_waitcnt vmcnt(4)\n\ts_barrier" ::: "memory");
        __builtin_amdgcn_raw_buffer_store_b64(u32x2{st1, tagb + T_ST}, rws, (lane & 47) == 0 ? st1off : 0xfffffff0u, 0, SC1);
      }
    }
    STAMP(3);
    u32x4 fb[16];
    #pragma unroll
    for (int s = 0; s < 16; ++s) fb[s] = *(LAS u32x4*)(tb + min(lane & 31, 20) * 528 + (lane >> 5) * 16 + s * 32);
    RELOAD();
    f32x2 x[4] = {f32x2{1.f, 0.f}, f32x2{1.f, 0.f}, z8[0], z8[1]};
    #pragma unroll
    for (int i = 0; i < 16; ++i) {
      f32x2 t[4]; unsigned pk[4];
      #pragma unroll
      for (int u = 0; u < 4; ++u) { pk[u] = pkh(x[u].x, x[u].y); GAP(); }
      if (i < 15) {
        #pragma unroll
        for (int u = 0; u < 4; ++u) { t[u] = x[u].xx * zs[u & 1]; GAP(); }
      }
      o[(2 * i) * 66] = u32x2{pk[0], pk[1]}; GAP();
      if (i < 15) {
        #pragma unroll
        for (int u = 0; u < 4; ++u) { x[u] = __builtin_elementwise_fma(x[u].yy, zsr[u & 1], t[u]); GAP(); }
      }
      o[(2 * i + 1) * 66] = u32x2{pk[2], pk[3]}; GAP();
      if (i < 5) ride(15 + i);
    }
    STAMP(4);
    fetch(0, 0); fetch(0, 1);
    f32x16 acc;
    {
      u32x4 fa[16];
      #pragma unroll
      for (int s = 0; s < 16; ++s) fa[s] = *(LAS u32x4*)(tb + (lane & 31) * 528 + (lane >> 5) * 16 + s * 32);
      asm volatile("v_mfma_f32_32x32x16_f16 %0, %1, %2, 0" : "=&v"(acc) : "v"(fa[0]), "v"(fb[0]));
      #pragma unroll
      for (int s = 1; s < 16; ++s) mh32_vv(acc, fa[s], fb[s]);
    }
    asm volatile("s_nop 10\n\ts_barrier" : "+v"(acc) :: "memory");
    if ((lane & 31) < 21) {
      #pragma unroll
      for (int i = 0; i < 4; ++i)
        *(LAS f32x4*)(lds + LA_RED + ((wv * 21 + (lane & 31)) * 36 + 8 * i + 4 * (lane >> 5)) * 4) = f32x4{acc[4 * i], acc[4 * i + 1], acc[4 * i + 2], acc[4 * i + 3]};
    }
    if (lane == 0) *(LAS unsigned*)(lds + LA_MISC + wv * 4) = eb;
    settle(0, 0); settle(0, 1);
    VMCLEAN();
    xload(0, 0, rws); xload(0, 1, rws);
    BAR();
    STAMP(5);
    st_flag_if(rws, b == 0 && tid == 0, WS_EPOCH, ep + 1u);
    w1_read<0, 10>(w, lds, w1rb);
    GAP();
    const u32x4 e4 = *(LAS u32x4*)(lds + LA_MISC);
    #pragma unroll
    for (int ch = 0; ch < 2; ++ch) {
      const unsigned ea = e4[2 * ch], ebb = e4[2 * ch + 1], em = max(ea, ebb);
      const float fa = em - ea > 120u ? 0.f : __builtin_bit_cast(float, (132u - (em - ea)) << 23);
      const float fb = em - ebb > 120u ? 0.f : __builtin_bit_cast(float, (132u - (em - ebb)) << 23);
      float k[8];
      #pragma unroll
      for (int j = 0; j < 8; ++j)
        k[j] = *(LAS float*)(lds + LA_RED + (2 * ch) * 3024 + kidx[j]) * fa + *(LAS float*)(lds + LA_RED + (2 * ch + 1) * 3024 + kidx[j]) * fb;
      *(LAS u32x4*)(lds + LA_KC + ch * 4160 + tid * 16) = u32x4{pkh(k[0], k[1]), pkh(k[2], k[3]), pkh(k[4], k[5]), pkh(k[6], k[7])};
      const float ys = (ch ? dt1 : dt0) * __builtin_bit_cast(float, em << 23) * 0.0625f;
      if (ch == 0 || (tid & 1)) ysc = ys;
    }
    if (tid < 8) *(LAS u32x4*)(lds + LA_KC + (tid >> 2) * 4160 + 4096 + (tid & 3) * 16) = u32x4{0, 0, 0, 0};
    w1_pin<0, 10>(w);
    normalise(0); normalise(1);
    BAR();
  }
  STAMP(6);

  constexpr unsigned xq = 16 * XB_SZ, hq = 32 * 57344;
  unsigned wvx = wv * 7168, xsrc = gam * XB_SZ + ((wv + sid) & 3) * 7168;
  asm volatile("" : "+s"(wvx), "+s"(xsrc));
  auto conv_out = [&](const int layer, const int half, f32x16& acc, auto&& after_bar) AI {
    const int tid = fresh(threadIdx.x), lane = tid & 63;
    asm volatile("s_nop 11" : "+v"(acc));
    lptr py = lds + LA_Y + ((wv & 1) * 2 + (wv >> 1)) * 4608 + (lane & 31) * 144 + 16 * (lane >> 5);
    #pragma unroll
    for (int i = 0; i < 4; ++i) *(LAS f32x4*)(py + 32 * i) = f32x4{acc[4 * i], acc[4 * i + 1], acc[4 * i + 2], acc[4 * i + 3]};
    BAR();
    after_bar();
    const lptr q = lds + LA_Y + (tid & 1) * 4608 + (tid >> 3) * 144 + ((tid >> 1) & 3) * 32;
    const f32x4 y0 = *(LAS f32x4*)(q) + *(LAS f32x4*)(q + 9216), y1 = *(LAS f32x4*)(q + 16) + *(LAS f32x4*)(q + 9232);
    const float* bs = base[half];
    const u32x4 v = {pkbf(__builtin_fmaf(y0[0], ysc, bs[0]), __builtin_fmaf(y0[1], ysc, bs[1])), pkbf(__builtin_fmaf(y0[2], ysc, bs[2]), __builtin_fmaf(y0[3], ysc, bs[3])),
                     pkbf(__builtin_fmaf(y1[0], ysc, bs[4]), __builtin_fmaf(y1[1], ysc, bs[5])), pkbf(__builtin_fmaf(y1[2], ysc, bs[6]), __builtin_fmaf(y1[3], ysc, bs[7]))};
    __builtin_amdgcn_raw_buffer_store_b128(v, rws, ((tid >> 3) * D_ + c0 + (tid & 1)) * 64 + ((tid >> 1) & 3) * 16, WS_X1 + layer * ACT_BYTES + half * 32 * D_ * 64, SC1);
  };
  auto conv = [&](auto layer_c) AI {
    constexpr int layer = decltype(layer_c)::value;
    const int lane = fresh(threadIdx.x) & 63;
    const unsigned fxoff = WS_FX + (28 * xg + idx) * 64, fxb = tagb + T_FX + 2 * layer;
    if (layer) {
      if (!f2_ok(0)) f2_wait(0);
      xload(1, 0, rws); fetch(1, 0);
      kt_build(lds, fresh(threadIdx.x));
      STAMP(7 + 24 * layer);
      settle(1, 0);
      STAMP(8 + 24 * layer);
      normalise(0);
    } else {
      #pragma unroll
      for (int n = 0; n < 4; ++n) w1_piece(rw1, w1vo, w1gb, w1m0, 10, n);
      kt_build(lds, fresh(threadIdx.x));
    }
    u_write(0);
    if (layer == 0) u_write(1);
    BAR();
    STAMP(10 + 24 * layer);
    const int ch = wv >> 1, d1 = 16 * (wv & 1) + 15;
    const lptr pa = lds + LA_KT + ch * KT_CH + (3 - (lane & 3)) * KT_CP + 4088 - 8 * ((lane & 31) >> 2) + 16 * (lane >> 5) - 64 * d1;
    const lptr pb = lds + LA_U + ch * U_CH + ((lane & 31) - d1 + 31) * 80 + 16 * (lane >> 5);
    f32x16 acc;
    conv_pass<true>(acc, pa, pb, [&](int n) AI { if (layer == 0 && n % 4 == 0) w1_piece(rw1, w1vo, w1gb, w1m0, 10, 4 + n / 4);
                                                 if (layer == 1 && n == 12) f2_load(); });
    STAMP(11 + 24 * layer);
    bool rdy = true;
    conv_out(layer, 0, acc, [&]() AI { if (layer == 1) { rdy = f2_ok(1); xload(1, 1, mk_rsrc(p.ws, rdy ? WS_TOTAL : 0)); } });
    STAMP(12 + 24 * layer);
    int nv = 0;
    conv_pass<true>(acc, pa - 2048, pb, [&](int n) AI {
      if (layer == 0 && n < 24 && n % 3 == 0) { w1_piece(rw1, w1vo, w1gb, w1m0, 10, 12 + n / 3); ++nv; }
      if (layer == 1 && n == 4) { fetch(1, 1); nv += 4; }
      if (n == 24) {
        vmwait(nv); asm volatile("s_barrier" ::: "memory");
        st_flag_if(rws, threadIdx.x == 0, fxoff, fxb + 1);
      }
    });
    if (layer == 0) {
      VMWAIT(1); asm volatile("s_barrier" ::: "memory");
      w1_read<10, 10>(w, lds, w1rb);
      w1_pin<10, 10>(w);
    } else {
      if (!rdy) { f2_wait(1); xload(1, 1, rws); VMCLEAN(); }
      settle(1, 1);
      normalise(1); u_write(1);
    }
    BAR();
    STAMP(13 + 24 * layer);
    const unsigned fxpoll = lane < 56 ? WS_FX + (56 * ((wv + sid) & 3) + lane) * 64 : 0xfffffff0u;
    const unsigned csvo = (((lane >> 3) & 1) * F_ + 128 * js + 32 * wv + 8 * ((lane >> 4) & 1) + (lane & 7)) * 8;
    unsigned fx, fw; u32x2 cs[2];
    auto cs_load = [&]() AI {
      #pragma unroll
      for (int t = 0; t < 2; ++t) cs[t] = __builtin_amdgcn_raw_buffer_load_b64(rws, csvo + t * 128, WS_CS, SC1);
      fw = __builtin_amdgcn_raw_buffer_load_b32(rws, (lane & 31) * 64, WS_FW, SC1);
    };
    conv_pass<false>(acc, pa, pb + 2560, [&](int n) AI {
      if (layer == 0 && n < 23 && n % 3 != 2) w1_piece(rw1, w1vo, w1gb, w1m0, 20, n - n / 3);
      if (layer == 0 && n == 24) cs_load();
      if (n == 25) fx = __builtin_amdgcn_raw_buffer_load_b32(rws, fxpoll, 0, SC1);
    });
    STAMP(9 + 24 * layer);
    const unsigned xso = WS_X1 + layer * ACT_BYTES + xsrc;
    auto x_tile = [&](int q) AI {
      set_m0(q * XB_SZ + wvx);
      dma<0, true>(lane * 16, rws, xso + q * xq); dma<1024, true>(lane * 16, rws, xso + q * xq);
      dma<2048, true>(lane * 16, rws, xso + q * xq); dma<3072, true>(lane * 16, rws, xso + q * xq);
      set_m0(q * XB_SZ + wvx + 4096);
      dma<0, true>(lane * 16, rws, xso + q * xq + 4096); dma<1024, true>(lane * 16, rws, xso + q * xq + 4096);
      dma<2048, true>(lane * 16, rws, xso + q * xq + 4096);
    };
    conv_out(layer, 1, acc, [&]() AI { asm volatile("" : "+v"(fx) :: "memory"); });
    const bool early = __all(lane >= 56 || (fx - (fxb + 1)) < 4u);
    if (early) x_tile(0);
    if (layer == 0) {
      if (!__all(cs[0].y == tagb + T_CS && cs[1].y == tagb + T_CS && fw == tagb + T_FW)) {
        #pragma unroll 1
        for (unsigned n = 0; n < SPIN_CAP; ++n) { RELOAD(); cs_load(); if (cs[0].y == tagb + T_CS && cs[1].y == tagb + T_CS && fw == tagb + T_FW) break; }
        VMCLEAN();
      }
      #pragma unroll
      for (int t = 0; t < 2; ++t) {
        const float a = asf(cs[t].x), h = rbf(a), m = rbf(a - h), l = a - h - m;
        w.WS[t] = lane < 32 ? u32x4{pkbf(h, m), pkbf(l, 0.f), 0, 0} : u32x4{0, 0, 0, 0};
      }
    }
    if (early) VMWAIT(7); else VMWAIT(0);
    BAR();
    st_flag_if(rws, threadIdx.x == 0, fxoff, fxb + 2);
    STAMP(14 + 24 * layer);
    if (!early) { poll_flags(p.ws, fxpoll, lane < 56, fxb + 1, 4); x_tile(0); x_tile(1); VMWAIT(7); } else { x_tile(1); VMWAIT(8); }
    STAMP(16 + 24 * layer);
    if (layer == 0) { w1_read<20, 8>(w, lds, w1rb); w1_pin<20, 8>(w); }
  };

  auto ffn = [&](auto layer_c) AI {
    constexpr int layer = decltype(layer_c)::value;
    const unsigned fhb = tagb + T_FH + 4 * layer;
    const int lane = fresh(threadIdx.x) & 63;
    const unsigned fhoff = WS_FH + (gam * 56 + 4 * js + wv) * 64;
    const unsigned fpoll = lane < 14 ? WS_FH + ((s2 >> 1) * 56 + 14 * wv + lane) * 64 : 0xfffffff0u;
    const unsigned fxpoll = lane < 56 ? WS_FX + (56 * ((wv + sid) & 3) + lane) * 64 : 0xfffffff0u;
    const unsigned hb = WS_H + layer * H_BYTES + s2 * 57344;
    u32x4 Bh[14]; unsigned pf; int nh = 0;
    {
      f32x16 A0[2], A1[2];
      const unsigned xso = WS_X1 + layer * ACT_BYTES + xsrc;
      const unsigned hvo = (((lane >> 4) & 1) * 56 + 4 * js + wv) * 1024 + (16 * (lane >> 5) + (lane & 15)) * 16;
      const unsigned hso = WS_H + layer * H_BYTES + 2 * gam * 57344;
      const unsigned stvo = (32 * gam + (lane & 31)) * 8, stso = WS_ST + (2 * layer + 1) * 16384, sttag = tagb + T_ST + 2 * layer + 1;
      const unsigned rb = (8 * (lane >> 5) + ((lane >> 2) & 3)) * 64 + ((lane >> 4) & 1) * 32 + (lane & 3) * 8;
      u32x4 B[3]; u32x2 stn; float c1 = 0.f, sd = 0.f; unsigned negm = 0;
      auto wd_ride = [&](int n) AI {
        if (n < 20) {
          const unsigned so = WS_WD + (cc * 56 + rot.k(9 + (n >> 2))) * 4096;
          if ((n & 3) == 0 || n == 14) set_m0(LA_W + wv * 20480 + (n >> 2) * 4096);
          if ((n & 3) == 0) dma<0, true>(lane * 16, rws, so);
          if ((n & 3) == 1) dma<1024, true>(lane * 16, rws, so);
          if ((n & 3) == 2) dma<2048, true>(lane * 16, rws, so);
          if ((n & 3) == 3) dma<3072, true>(lane * 16, rws, so);
        } else {
          const int m = n - 20;
          const u32x4 f = __builtin_amdgcn_raw_buffer_load_b128(rws, lane * 16 + (m & 3) * 1024, WS_WD + (cc * 56 + rot.k(m >> 2)) * 4096, SC1);
          if (m < 8) w.WdA[m] = f; else w.WdV[m - 8] = f;
        }
        return 1;
      };
      auto h_first = [&](int n) AI { Bh[G2_ORD[n]] = __builtin_amdgcn_raw_buffer_load_b128(rws, lane * 16, hb + rot.k(G2_ORD[n]) * 1024, SC1); return 1; };
      asm volatile("s_barrier" ::: "memory");
      STAMP(17 + 24 * layer);
      #pragma unroll
      for (int s = 0; s < 2; ++s) B[s] = cat(trr(lds + rb + s * 1024), trr(lds + rb + s * 1024 + 256));
      g1_step<0>(w, lds, A0, A1, B, rb, rb + XB_SZ, c1, sd, negm, rws, hvo, hso, lane * 16, 0, 0, stn, stvo, stso, p.ws, fhoff, 0, lane, nh, pf, fxpoll,
                 [&](int s) AI { return layer == 0 && !(s & 1) ? wd_ride(s >> 1) : 0; });
      { u32x4 t = B[0]; B[0] = B[1]; B[1] = B[2]; B[2] = t; }
      stats_take(stn, rws, stvo, stso, sttag, c1, sd, negm);
      STAMP(18 + 24 * layer);
      g1_step<1>(w, lds, A1, A0, B, rb + XB_SZ, rb, c1, sd, negm, rws, hvo, hso, lane * 16, wvx, xso + 2 * xq, stn, stvo, stso + 4096, p.ws, fhoff, tagb + T_FX + 2 * layer + 2, lane, nh, pf, fxpoll,
                 [&](int s) AI { return layer == 0 && s >= 13 && s < 27 ? wd_ride(14 + s - 13) : 0; });
      { u32x4 t = B[0]; B[0] = B[1]; B[1] = B[2]; B[2] = t; }
      stats_take(stn, rws, stvo, stso + 4096, sttag, c1, sd, negm);
      STAMP(19 + 24 * layer);
      g1_step<2>(w, lds, A0, A1, B, rb, rb + XB_SZ, c1, sd, negm, rws, hvo, hso + hq, lane * 16, XB_SZ + wvx, xso + 3 * xq, stn, stvo, stso + 8192, p.ws, fhoff, fhb + 1, lane, nh, pf, fxpoll,
                 [&](int s) AI { return layer == 0 && s >= 7 && s % 3 != 0 ? wd_ride(28 + s - 7 - (s - 6) / 3) : 0; });
      { u32x4 t = B[0]; B[0] = B[1]; B[1] = B[2]; B[2] = t; }
      stats_take(stn, rws, stvo, stso + 8192, sttag, c1, sd, negm);
      STAMP(20 + 24 * layer);
      g1_step<3>(w, lds, A1, A0, B, rb + XB_SZ, rb, c1, sd, negm, rws, hvo, hso + 2 * hq, lane * 16, 0, 0, stn, stvo, stso + 12288, p.ws, fhoff, fhb + 2, lane, nh, pf, fxpoll,
                 [&](int s) AI { if (s == 14) { pf = __builtin_amdgcn_raw_buffer_load_b32(rws, fpoll, 0, SC1); return 1; }
                                 if (s == 23) {
                                   asm volatile("" : "+v"(pf) :: "memory");
                                   if (!__all(lane >= 14 || (pf - (fhb + 1)) < 8u)) poll_flags(p.ws, fpoll, lane < 14, fhb + 1, 8);
                                 }
                                 if (s >= 23) return h_first(s - 23);
                                 return layer == 0 && s < 21 && s % 3 != 2 ? wd_ride(42 + s - (s + 1) / 3) : 0; });
      stats_take(stn, rws, stvo, stso + 12288, sttag, c1, sd, negm);
      STAMP(21 + 24 * layer);
      RELOAD();
      pf = __builtin_amdgcn_raw_buffer_load_b32(rws, fpoll, 0, SC1); ++nh;
      g1_step<4>(w, lds, A0, A1, B, 0, 0, c1, sd, negm, rws, hvo, hso + 3 * hq, 0, 0, 0, stn, 0, 0, p.ws, fhoff, fhb + 3, lane, nh, pf, fxpoll,
                 [&](int s) AI { return s >= 9 && s < 27 && (s & 1) ? h_first(5 + ((s - 9) >> 1)) : 0; });
    }
    STAMP(22 + 24 * layer);
    const int ech = layer ? lane : 16 * wv + (lane >> 2), etq = layer ? wv : lane & 3;
    constexpr unsigned PW = layer ? PT_W : PT_W0, PP = layer ? 20 : 16;
    const unsigned rvo = ((s2 >> 1) * D_ + 64 * cc + ech) * 64 + ((s2 & 1) * 16 + 4 * etq) * 2;
    const lptr wl = lds + LA_W + wv * 20480 + lane * 16;
    const unsigned f2off = WS_F2 + ((s2 * 7 + cc) * 4 + wv) * 64;
    if (layer == 0) {
      #pragma unroll
      for (int i = 0; i < 8; ++i) asm volatile("" : "+a"(w.WdA[i]));
    }
    asm volatile("s_barrier" ::: "memory");
    STAMP(23 + 24 * layer);
    int n2 = 0;
    u32x2 res; f32x4 y[4]; unsigned o0, o1; float x[4], r[4], t0, t1;
    auto epi = [&](int q, int ph) AI {
      const unsigned S = 32 * q + s2;
      const lptr pr2 = lds + (q & 1) * PT_SZ + ech * PP * 4 + etq * 16;
      if (ph == 0) BAR();
      if (ph == 1) { y[0] = *(LAS f32x4*)(pr2); y[1] = *(LAS f32x4*)(pr2 + PW); y[2] = *(LAS f32x4*)(pr2 + 2 * PW); y[3] = *(LAS f32x4*)(pr2 + 3 * PW); }
      if (ph == 2) { y[0] += y[1]; y[2] += y[3]; }
      if (ph == 3) { y[0] += y[2]; y[0] += f32x4{bflo(res.x), bfhi(res.x), bflo(res.y), bfhi(res.y)}; }
      if (ph == 4) { o0 = pkbf(y[0][0], y[0][1]); o1 = pkbf(y[0][2], y[0][3]);
        if (layer == 0) { __builtin_amdgcn_raw_buffer_store_b64(u32x2{o0, o1}, rws, (64 * cc + 16 * wv) * 32 + lane * 8, WS_XT + ACT_BYTES + S * D_ * 32, SC1); ++nh; n2 = 0; } }
      if (layer == 0) {
        if (ph == 5) { x[0] = bflo(o0); x[1] = bfhi(o0); x[2] = bflo(o1); x[3] = bfhi(o1); r[0] = x[0] * x[0]; r[1] = x[1] * x[1]; r[2] = x[2] * x[2]; r[3] = x[3] * x[3]; }
        if (ph == 6) { r[0] = sw32_add(x[0], r[0]); r[1] = sw32_add(x[1], r[1]); r[2] = sw32_add(x[2], r[2]); r[3] = sw32_add(x[3], r[3]); }
        if (ph == 7) { t0 = sw16_add(r[0], r[1]); t1 = sw16_add(r[2], r[3]); }
        if (ph == 8) { t0 = dpp_add<DPP_ROR4>(t0); t1 = dpp_add<DPP_ROR4>(t1); GAP(); t0 = dpp_add<DPP_ROR8>(t0); t1 = dpp_add<DPP_ROR8>(t1); }
        if (ph == 9) {
          const unsigned go = (lane & 8) == 0 ? ((4 * (lane & 3) + 2 * ((lane >> 2) & 1) + ((lane >> 4) & 1)) * 2 + (lane >> 5)) * 8 : 0xfffffff0u;
          __builtin_amdgcn_raw_buffer_store_b64(u32x2{__builtin_bit_cast(unsigned, (lane & 4) ? t1 : t0), tagb + T_PS}, rws, go, WS_PS + (S * 28 + 4 * cc + wv) * 256, SC1); ++nh; ++n2;
        }
      } else if (ph >= 5 && ph < 9) {
        GAS u16* o = (GAS u16*)p.out + (16 * S + 4 * wv + (ph - 5)) * D_ + 64 * cc + lane;
        *o = (u16)(ph == 5 ? o0 : ph == 6 ? o0 >> 16 : ph == 7 ? o1 : o1 >> 16); ++nh;
      }
    };
    sfor<0, 4>([&](auto qc) AI {
      constexpr int q = decltype(qc)::value;
      bool ready = false; rsrc_t rh;
      if (q == 1 || q == 2) { pf = __builtin_amdgcn_raw_buffer_load_b32(rws, fpoll, 0, SC1); ++nh; ++n2; }
      const unsigned hn = hb + (q + 1) * hq;
      u32x4 wf[4];
      #pragma unroll
      for (int t = 0; t < 4; ++t) wf[t] = *(LAS u32x4*)(wl + t * 1024);
      GAP();
      f32x4 a[4];
      sfor<0, 14>([&](auto sc) AI {
        constexpr int s = decltype(sc)::value, o = G2_ORD[s];
        #pragma unroll
        for (int t = 0; t < 4; ++t) {
          if (o == 0) mf16_a0(a[t], w.WdA[t], Bh[0]);
          else if (o == 1) mf16_a(a[t], w.WdA[4 + t], Bh[1]);
          else if (o < 9) mf16_v(a[t], w.WdV[(4 * (o - 2) + t) % 28], Bh[o]);
          else { mf16_v(a[t], wf[t], Bh[o]); if (o < 13) { GAP(); wf[t] = *(LAS u32x4*)(wl + (4 * (o - 8) + t) * 1024); } }
          GAP();
          if (q > 0 && (t & 1)) {
            const int ph = s == 1 ? (t >> 1) : s >= 3 ? 2 * (s - 2) + (t >> 1) : -1;
            if (ph >= 0 && ph < 10) { epi(q - 1, ph); GAP(); }
          }
        }
        if (q < 3 && s == (q ? 6 : 0)) {
          asm volatile("" : "+v"(pf) :: "memory");
          ready = __all(lane >= 14 || (pf - (fhb + q + 2)) < 8u);
          rh = mk_rsrc(p.ws, ready ? WS_TOTAL : 0);
        }
        if (q < 3 && (s >= 6 || q == 0)) {
          constexpr int lo = q && s < 12 ? 2 * (s - 6) : s, hi = q && s < 12 ? lo + 2 : s + 1;
          #pragma unroll
          for (int u = lo; u < hi; ++u) { Bh[G2_ORD[u]] = __builtin_amdgcn_raw_buffer_load_b128(rh, lane * 16, hn + rot.k(G2_ORD[u]) * 1024, SC1); ++nh; ++n2; GAP(); }
        }
        if (s == 12) { res = __builtin_amdgcn_raw_buffer_load_b64(rws, rvo, WS_X1 + layer * ACT_BYTES + q * xq, SC1); ++nh; ++n2; }
        if (layer == 0 && q == 2 && s == 13) { vmwait(n2); st_flag_if(rws, lane == 0, f2off, tagb + T_F2 + 2); }
        if (s == 8 && q == 0) { vmwait(nh); st_flag_if(rws, lane == 0, fhoff, fhb + 4); }
        GAP();
      });
      asm volatile("s_nop 7" : "+v"(a[0]), "+v"(a[1]), "+v"(a[2]), "+v"(a[3]));
      {
        LAS float* o = (LAS float*)(lds + (q & 1) * PT_SZ + wv * PW) + (32 * ((lane >> 4) & 1) + 4 * (lane >> 5)) * PP + (lane & 15);
        #pragma unroll
        for (int t = 0; t < 4; ++t)
          #pragma unroll
          for (int e = 0; e < 4; ++e) o[(8 * e + t) * PP] = a[t][e];
      }
      GAP();
      if (q < 3 && !ready) {
        poll_flags(p.ws, fpoll, lane < 14, fhb + q + 2, 8);
        #pragma unroll
        for (int s = 0; s < 14; ++s) { Bh[G2_ORD[s]] = __builtin_amdgcn_raw_buffer_load_b128(rws, lane * 16, hn + rot.k(G2_ORD[s]) * 1024, SC1); GAP(); }
      }
      STAMP(24 + 24 * layer + q);
    });
    if (layer == 0) f2_load();
    #pragma unroll
    for (int ph = 0; ph < 10; ++ph) epi(3, ph);
    if (layer == 0) {
      VMCLEAN();
      st_flag_if(rws, lane == 0, f2off, tagb + T_F2 + 4);
      asm volatile("s_barrier" ::: "memory");
    }
    STAMP(28 + 24 * layer);
  };

  conv(std::integral_constant<int, 0>{});
  ffn(std::integral_constant<int, 0>{});
  conv(std::integral_constant<int, 1>{});
  ffn(std::integral_constant<int, 1>{});
  STAMP_OUT();
  asm volatile(".Lfused_code_end:");
}

}

extern "C" void kernel_launch(void* const* d_in, const int* in_sizes, int n_in, void* d_out, int out_size, void* d_ws, size_t ws_size,
                              hipStream_t stream) {
  Params p;
  p.u = (const u16*)d_in[0]; p.lar = (const u16*)d_in[1]; p.aim = (const u16*)d_in[2]; p.cre = (const u16*)d_in[3];
  p.cim = (const u16*)d_in[4]; p.ldt = (const u16*)d_in[5]; p.dsk = (const u16*)d_in[6];
  p.wg = (const u16*)d_in[8]; p.wu = (const u16*)d_in[9]; p.wd = (const u16*)d_in[10];
  p.out = (u16*)d_out; p.ws = (unsigned char*)d_ws;
  (void)hipFuncSetAttribute((const void*)fused_kernel, hipFuncAttributeMaxDynamicSharedMemorySize, LDS_BYTES);
  hipLaunchKernelGGL(fused_kernel, dim3(256), dim3(256), LDS_BYTES, stream, p);
}
```

```cpp
#include <hip/hip_runtime.h>
#include <hip/hip_bf16.h>
#include <type_traits>
#include <utility>

namespace {

#define GAS __attribute__((address_space(1)))
#define LAS __attribute__((address_space(3)))
#define DI __device__ __forceinline__
#define AI __attribute__((always_inline))
#define RARE(c) __builtin_expect(!!(c), 0)
#define GAP() __builtin_amdgcn_sched_barrier(0)
#define RLX_AGENT __ATOMIC_RELAXED, __HIP_MEMORY_SCOPE_AGENT
typedef float f32x16 __attribute__((ext_vector_type(16)));
typedef float f32x4 __attribute__((ext_vector_type(4)));
typedef float f32x2 __attribute__((ext_vector_type(2)));
typedef __bf16 bf16x2 __attribute__((ext_vector_type(2)));
typedef _Float16 f16x2 __attribute__((ext_vector_type(2)));
typedef short s16x4 __attribute__((ext_vector_type(4)));
typedef unsigned u32x4 __attribute__((ext_vector_type(4)));
typedef unsigned u32x2 __attribute__((ext_vector_type(2)));
typedef __amdgpu_buffer_rsrc_t rsrc_t;
typedef LAS unsigned char* lptr;
typedef unsigned short u16;

constexpr int L_ = 2048, D_ = 448, N_ = 256, F_ = 1792;
constexpr int SC1 = 16;
constexpr int SLH = 8;
constexpr unsigned SPIN_CAP = 1u << 17, RETRY_CAP = 1u << 12;
constexpr unsigned EMPTY = 0xffffffffu;

constexpr unsigned WS_EPOCH = 0;
constexpr unsigned WS_ST = 4096;
constexpr unsigned WS_PS = WS_ST + 65536;
constexpr unsigned WS_CS = WS_PS + 128 * 28 * 32 * 8;
constexpr unsigned WS_FW = WS_CS + 2 * F_ * 8;
constexpr unsigned WS_CTL_END = WS_FW + 32 * 64;
constexpr unsigned ACT_BYTES = L_ * D_ * 2;
constexpr unsigned WS_XT = 2u << 20;
constexpr unsigned WS_X1 = WS_XT + 2 * ACT_BYTES;
constexpr unsigned H_BYTES = L_ * F_ * 2;
constexpr unsigned WS_H = WS_X1 + 2 * ACT_BYTES;
constexpr unsigned WS_WD = WS_H + 2 * H_BYTES;
constexpr unsigned WS_DBG = WS_WD + D_ * F_ * 2;
constexpr unsigned WS_TOTAL = WS_DBG + (1u << 20);
static_assert(WS_CTL_END <= WS_XT, "control block");

constexpr unsigned T_ST = 64, T_PS = 80, T_CS = 96, T_FW = 97;

constexpr unsigned LA_U = 128;
constexpr unsigned U_CH = 7600;
constexpr unsigned LA_KT = 15360;
constexpr unsigned KT_CP = 4160, KT_CH = 4 * KT_CP;
constexpr unsigned LA_Y = 48640;
constexpr unsigned XB_SZ = 28672;
constexpr unsigned PT_W = 5120, PT_W0 = 4096, PT_SZ = 4 * PT_W;
constexpr unsigned TB_W = 32 * 528;
constexpr unsigned LA_RED = 57856;
constexpr unsigned LA_MISC = 69952;
constexpr unsigned LA_DUMP = 70656;
constexpr unsigned LA_HS = 8192;
constexpr unsigned LA_KC = 73216;
constexpr unsigned LA_W = 81664;
constexpr unsigned W_PL = 40960;
constexpr unsigned LDS_BYTES = LA_W + 81920;
static_assert(LDS_BYTES <= 163840 && 4 * TB_W <= LA_MISC && LA_RED + 4 * 21 * 36 * 4 <= LA_MISC, "lds map");

struct Params {
  const u16 *u, *lar, *aim, *cre, *cim, *ldt, *dsk, *wg, *wu, *wd;
  u16* out;
  unsigned char* ws;
};

DI rsrc_t mk_rsrc(const void* p, unsigned bytes) { return __builtin_amdgcn_make_buffer_rsrc((void*)p, 0, (int)bytes, 0x00020000); }
DI float asf(unsigned x) { return __builtin_bit_cast(float, x); }
DI float bflo(unsigned x) { return __builtin_bit_cast(float, x << 16); }
DI float bfhi(unsigned x) { return __builtin_bit_cast(float, x & 0xffff0000u); }
DI unsigned pkbf(float lo, float hi) { f32x2 v = {lo, hi}; return __builtin_bit_cast(unsigned, __builtin_convertvector(v, bf16x2)); }
DI unsigned pkbf_s(float lo, float hi) { unsigned r; asm("v_cvt_pk_bf16_f32 %0, %1, %2" : "=v"(r) : "v"(lo), "v"(hi)); return r; }
DI unsigned pkh(float lo, float hi) { f32x2 v = {lo, hi}; return __builtin_bit_cast(unsigned, __builtin_convertvector(v, f16x2)); }
DI float fadd_s(float a, float b) { float r; asm("v_add_f32_e32 %0, %1, %2" : "=v"(r) : "v"(a), "v"(b)); return r; }
DI bool all_seen(unsigned v, unsigned want, unsigned win, unsigned long long lanes) { return (__builtin_amdgcn_ballot_w64((v - want) >= win) & lanes) == 0; }
DI unsigned umax4(unsigned a, const u32x4& v) { return max(max(max(a, v.x), v.y), max(v.z, v.w)); }
DI bool any_empty(unsigned m) { return __builtin_amdgcn_ballot_w64(m == EMPTY) != 0; }
DI float rbf(float x) { return bflo(pkbf(x, x) & 0xffffu); }
DI u32x2 trr(lptr p) { return __builtin_bit_cast(u32x2, __builtin_amdgcn_ds_read_tr16_b64_v4i16((LAS s16x4*)p)); }
DI u32x4 cat(u32x2 a, u32x2 b) { return u32x4{a.x, a.y, b.x, b.y}; }
DI unsigned ld_flag(unsigned char* ws, unsigned off) { return __hip_atomic_load((GAS unsigned*)(ws + off), RLX_AGENT); }
DI void st_flag(unsigned char* ws, unsigned off, unsigned v) { __hip_atomic_store((GAS unsigned*)(ws + off), v, RLX_AGENT); }
DI void st_flag_if(rsrc_t rws, bool c, unsigned off, unsigned v) { __builtin_amdgcn_raw_buffer_store_b32(v, rws, c ? off : 0xfffffff0u, 0, SC1); }
DI void st_gran(unsigned char* ws, unsigned off, unsigned val, unsigned tag) {
  __hip_atomic_store((GAS unsigned long long*)(ws + off), ((unsigned long long)tag << 32) | val, RLX_AGENT);
}
#define BAR() asm volatile("s_waitcnt lgkmcnt(0)\n\ts_barrier" ::: "memory")
#define VMWAIT(n) asm volatile("s_waitcnt vmcnt(" #n ")" ::: "memory")
#define ORDER() __builtin_amdgcn_fence(__ATOMIC_ACQUIRE, "wavefront")

template <int CTRL> DI float dpp_add(float x) {
  return x + __builtin_bit_cast(float, __builtin_amdgcn_update_dpp(0, __builtin_bit_cast(int, x), CTRL, 0xf, 0xf, true));
}
template <int CTRL> DI unsigned dpp_umax(unsigned x) {
  unsigned y = (unsigned)__builtin_amdgcn_update_dpp(0, (int)x, CTRL, 0xf, 0xf, true); return x > y ? x : y;
}
constexpr int DPP_X1 = 0xB1, DPP_X2 = 0x4E, DPP_ROR4 = 0x124, DPP_ROR8 = 0x128;
DI float sw32_add(float a, float b) {
  auto r = __builtin_amdgcn_permlane32_swap(__builtin_bit_cast(unsigned, a), __builtin_bit_cast(unsigned, b), false, false);
  return asf(r[0]) + asf(r[1]);
}
DI float sw16_add(float a, float b) {
  auto r = __builtin_amdgcn_permlane16_swap(__builtin_bit_cast(unsigned, a), __builtin_bit_cast(unsigned, b), false, false);
  return asf(r[0]) + asf(r[1]);
}
DI float row_sum(float x) { return dpp_add<DPP_ROR8>(dpp_add<DPP_ROR4>(dpp_add<DPP_X2>(dpp_add<DPP_X1>(x)))); }
DI unsigned ln_pack(float s, float q) {
  const float m = s * (1.0f / D_);
  float v = q * (1.0f / D_) - m * m; v = v > 0.f ? v : 0.f;
  const float sd = __builtin_sqrtf(rbf(rbf(v) + 1e-5f));
  return pkbf(m, sd);
}

DI void mf32_first(f32x16& d, const u32x4& a, const u32x4& b) { asm volatile("v_mfma_f32_32x32x16_bf16 %0, %1, %2, 0" : "=&v"(d) : "a"(a), "v"(b)); }
DI void mf32_acc(f32x16& d, const u32x4& a, const u32x4& b) { asm volatile("v_mfma_f32_32x32x16_bf16 %0, %1, %2, %0" : "+v"(d) : "a"(a), "v"(b)); }
DI void mf32_vv(f32x16& d, const u32x4& a, const u32x4& b) { asm volatile("v_mfma_f32_32x32x16_bf16 %0, %1, %2, %0" : "+v"(d) : "v"(a), "v"(b)); }
DI void mf32_va(f32x16& d, const u32x4& a, const u32x4& b) { asm volatile("v_mfma_f32_32x32x16_bf16 %0, %1, %2, %0" : "+v"(d) : "v"(a), "a"(b)); }
DI void mh32_vv(f32x16& d, const u32x4& a, const u32x4& b) { asm volatile("v_mfma_f32_32x32x16_f16 %0, %1, %2, %0" : "+v"(d) : "v"(a), "v"(b)); }
DI void mf16_a0(f32x4& d, const u32x4& a, const u32x4& b) { asm volatile("v_mfma_f32_16x16x32_bf16 %0, %1, %2, 0" : "=&v"(d) : "a"(a), "v"(b)); }
DI void mf16_a(f32x4& d, const u32x4& a, const u32x4& b) { asm volatile("v_mfma_f32_16x16x32_bf16 %0, %1, %2, %0" : "+v"(d) : "a"(a), "v"(b)); }
DI void mf16_v(f32x4& d, const u32x4& a, const u32x4& b) { asm volatile("v_mfma_f32_16x16x32_bf16 %0, %1, %2, %0" : "+v"(d) : "v"(a), "v"(b)); }

template <int IMM, bool COH> DI void dma(unsigned voff, rsrc_t rs, unsigned soff) {
  if (COH) asm volatile("buffer_load_dwordx4 %0, %1, %2 offen offset:%3 sc1 lds" :: "v"(voff), "s"(rs), "s"(soff), "i"(IMM) : "memory");
  else asm volatile("buffer_load_dwordx4 %0, %1, %2 offen offset:%3 lds" :: "v"(voff), "s"(rs), "s"(soff), "i"(IMM) : "memory");
}
DI void reload128(u32x4& d, rsrc_t rs, unsigned voff, unsigned soff) {
  asm volatile("buffer_load_dwordx4 %0, %1, %2, %3 offen sc1" : "+v"(d) : "v"(voff), "s"(rs), "s"(soff) : "memory");
}
DI void set_m0(unsigned v) { asm volatile("s_mov_b32 m0, %0\n\ts_nop 0" :: "s"(v) : "memory"); }

DI int fresh(int x) { asm volatile("" : "+v"(x)); return x; }
#define PADMASK 0ull
#define PHASE(i) do { if ((PADMASK >> (i)) & 1) asm volatile(".p2align 3\n\ts_nop 0"); else asm volatile(".p2align 3"); } while (0)
#define STAMP(i) PHASE(i)
#define STAMP_DECL do {} while (0)
#define STAMP_OUT() do {} while (0)

#define VMCLEAN() do { VMWAIT(0); __builtin_amdgcn_s_waitcnt(0x0F70); } while (0)
#define RELOAD() asm volatile("" ::: "memory")
template <int SL = 0> DI void poll_flags(unsigned char* ws, unsigned off, bool active, unsigned want, unsigned win) {
  if (active) {
    #pragma unroll 1
    for (unsigned n = 0; n < SPIN_CAP; ++n) {
      if ((ld_flag(ws, off) - want) < win) break;
      if (SL) __builtin_amdgcn_s_sleep(SL);
    }
  }
  VMCLEAN();
}

DI u32x4 wd_load(rsrc_t rwd, unsigned vo, int kstep, int j) { return __builtin_amdgcn_raw_buffer_load_b128(rwd, vo + j * 896, kstep * 32 * 896, 0); }
DI void wd_conv(const u32x4* r, u32x4* f) {
  unsigned lo[4][2], hi[4][2];
  #pragma unroll
  for (int j = 0; j < 4; ++j)
    #pragma unroll
    for (int i = 0; i < 2; ++i) {
      const unsigned a = r[j][i], b = r[j][2 + i];
      lo[j][i] = (unsigned)__builtin_amdgcn_update_dpp((int)a, (int)b, DPP_ROR8, 0xf, 0xc, false);
      hi[j][i] = (unsigned)__builtin_amdgcn_update_dpp((int)b, (int)a, DPP_ROR8, 0xf, 0x3, false);
    }
  #pragma unroll
  for (int t = 0; t < 4; ++t) {
    const unsigned sel = (t & 1) ? 0x07060302u : 0x05040100u;
    f[t] = u32x4{__builtin_amdgcn_perm(lo[1][t >> 1], lo[0][t >> 1], sel), __builtin_amdgcn_perm(lo[3][t >> 1], lo[2][t >> 1], sel),
                 __builtin_amdgcn_perm(hi[1][t >> 1], hi[0][t >> 1], sel), __builtin_amdgcn_perm(hi[3][t >> 1], hi[2][t >> 1], sel)};
  }
  asm volatile("" : "+v"(f[0]), "+v"(f[1]), "+v"(f[2]), "+v"(f[3]));
  GAP();
}
template <int B, class F, int... I> DI void sfor_(F&& f, std::integer_sequence<int, I...>) { (f(std::integral_constant<int, B + I>{}), ...); }
template <int B, int E, class F> DI void sfor(F&& f) { sfor_<B>(f, std::make_integer_sequence<int, E - B>{}); }
DI void vmwait(int n) {
  switch (n) {
    case 0: VMWAIT(0); break;
    case 1: VMWAIT(1); break;
    case 2: VMWAIT(2); break;
    case 3: VMWAIT(3); break;
    case 4: VMWAIT(4); break;
    case 5: VMWAIT(5); break;
    case 6: VMWAIT(6); break;
    case 7: VMWAIT(7); break;
    case 8: VMWAIT(8); break;
    case 9: VMWAIT(9); break;
    case 10: VMWAIT(10); break;
    case 11: VMWAIT(11); break;
    case 12: VMWAIT(12); break;
    case 13: VMWAIT(13); break;
    case 14: VMWAIT(14); break;
    case 15: VMWAIT(15); break;
    case 16: VMWAIT(16); break;
    case 17: VMWAIT(17); break;
    case 18: VMWAIT(18); break;
    case 19: VMWAIT(19); break;
    case 20: VMWAIT(20); break;
    case 21: VMWAIT(21); break;
    case 22: VMWAIT(22); break;
    case 23: VMWAIT(23); break;
    case 24: VMWAIT(24); break;
    case 25: VMWAIT(25); break;
    case 26: VMWAIT(26); break;
    case 27: VMWAIT(27); break;
    case 28: VMWAIT(28); break;
    case 29: VMWAIT(29); break;
    case 30: VMWAIT(30); break;
    case 31: VMWAIT(31); break;
    case 32: VMWAIT(32); break;
    case 33: VMWAIT(33); break;
    case 34: VMWAIT(34); break;
    case 35: VMWAIT(35); break;
    case 36: VMWAIT(36); break;
    case 37: VMWAIT(37); break;
    case 38: VMWAIT(38); break;
    case 39: VMWAIT(39); break;
    case 40: VMWAIT(40); break;
    default: VMWAIT(0); break;
  }
}

DI void helper_main(const Params& p, lptr lds, unsigned tagb, int xg, int hp, int lane, int wv) {
  const rsrc_t rws = mk_rsrc(p.ws, WS_TOTAL);
  STAMP_DECL;
  STAMP(0);
  {
    const int hid = 4 * xg + hp;
    const rsrc_t rwd = mk_rsrc(p.wd, D_ * F_ * 2);
    const unsigned vo = (16 * (lane >> 5) + 4 * ((lane >> 4) & 1) + 8 * ((lane >> 3) & 1)) * 896 + (lane & 7) * 16;
    u32x4 raw[16];
    #pragma unroll
    for (int i = 0; i < 4; ++i) {
      const int n = min(hid + 32 * (wv + 4 * i), 391), ct = n / 56;
      #pragma unroll
      for (int j = 0; j < 4; ++j) raw[4 * i + j] = wd_load(rwd, vo + ct * 128, n - 56 * ct, j);
    }
    #pragma unroll
    for (int i = 0; i < 4; ++i) {
      const int n = hid + 32 * (wv + 4 * i);
      u32x4 f[4]; wd_conv(&raw[4 * i], f);
      if (n < 392) {
        #pragma unroll
        for (int t = 0; t < 4; ++t) __builtin_amdgcn_raw_buffer_store_b128(f[t], rws, lane * 16 + t * 1024, WS_WD + n * 4096, SC1);
      }
    }
    VMCLEAN(); BAR();
    if (wv == 0 && lane == 0) st_flag(p.ws, WS_FW + hid * 64, tagb + T_FW);
    STAMP(1);
  }
  {
    const int hid = 4 * xg + hp, tid = 64 * wv + lane, co = tid % 14, rg = tid / 14;
    const rsrc_t rw = mk_rsrc(hid < 16 ? p.wg : p.wu, D_ * F_ * 2);
    const unsigned vo = rg < 16 ? rg * 28 * F_ * 2 + ((hid & 15) * 112 + co * 8) * 2 : 0xfffffff0u;
    f32x2 a[4] = {};
    #pragma unroll 1
    for (int r0 = 0; r0 < 28; r0 += 14) {
      u32x4 v[14];
      #pragma unroll
      for (int r = 0; r < 14; ++r) v[r] = __builtin_amdgcn_raw_buffer_load_b128(rw, vo, (r0 + r) * F_ * 2, 0);
      #pragma unroll
      for (int r = 0; r < 14; ++r)
        #pragma unroll
        for (int d = 0; d < 4; ++d) a[d] += f32x2{bflo(v[r][d]), bfhi(v[r][d])};
    }
    if (rg < 16) {
      *(LAS f32x4*)(lds + rg * 448 + co * 32) = f32x4{a[0].x, a[0].y, a[1].x, a[1].y};
      *(LAS f32x4*)(lds + rg * 448 + co * 32 + 16) = f32x4{a[2].x, a[2].y, a[3].x, a[3].y};
    }
    BAR();
    if (tid < 112) {
      float t = 0.f;
      #pragma unroll
      for (int r = 0; r < 16; ++r) t += *(LAS float*)(lds + r * 448 + tid * 4);
      st_gran(p.ws, WS_CS + (hid * 112 + tid) * 8, __builtin_bit_cast(unsigned, t), tagb + T_CS);
    }
  }
  STAMP(2);
  poll_flags<SLH>(p.ws, WS_ST + (64 * wv + lane) * 64 + 4, true, tagb + T_ST, 1);
  BAR();
  #pragma unroll 1
  for (int layer = 0; layer < 2; ++layer) {
    const unsigned hid = 4 * xg + hp;
    #pragma unroll 1
    for (int half = 0; half < 2; ++half) {
      const unsigned xo = WS_X1 + layer * ACT_BYTES + (32 * half + hid) * XB_SZ + wv * 7168;
      if (lane == 0) {
        #pragma unroll 1
        for (unsigned n = 0; n < SPIN_CAP; ++n) { if (ld_flag(p.ws, xo + hid * 128) != EMPTY) break; __builtin_amdgcn_s_sleep(SLH); }
      }
      STAMP(3 + 8 * layer + 2 * half);
      u32x4 v[7];
      #pragma unroll 1
      for (unsigned n = 0; n < RETRY_CAP; ++n) {
        RELOAD();
        unsigned m = 0;
        #pragma unroll
        for (int i = 0; i < 7; ++i) v[i] = __builtin_amdgcn_raw_buffer_load_b128(rws, lane * 16 + i * 1024, xo, SC1);
        #pragma unroll
        for (int i = 0; i < 7; ++i) m = umax4(m, v[i]);
        if (!any_empty(m)) break;
        __builtin_amdgcn_s_sleep(SLH);
      }
      f32x2 s[4] = {}, sq[4] = {};
      #pragma unroll
      for (int i = 0; i < 7; ++i)
        #pragma unroll
        for (int d = 0; d < 4; ++d) { f32x2 x = {bflo(v[i][d]), bfhi(v[i][d])}; s[d] += x; sq[d] = __builtin_elementwise_fma(x, x, sq[d]); }
      float w[4];
      #pragma unroll
      for (int d = 0; d < 4; ++d) w[d] = dpp_add<DPP_ROR8>(dpp_add<DPP_ROR4>(sw16_add(sw32_add(s[d].x, sq[d].x), sw32_add(s[d].y, sq[d].y))));
      if ((lane & 12) == 0) {
        LAS float* o = (LAS float*)(lds + LA_HS + half * 1024 + wv * 256 + (lane >> 5) * 128) + 8 * (lane & 3) + ((lane >> 4) & 1);
        #pragma unroll
        for (int d = 0; d < 4; ++d) o[2 * d] = w[d];
      }
      BAR();
      if (wv == 0 && lane < 32) {
        LAS float* o = (LAS float*)(lds + LA_HS + half * 1024) + lane;
        const float a = (o[0] + o[64]) + (o[128] + o[192]), b = (o[32] + o[96]) + (o[160] + o[224]);
        st_gran(p.ws, WS_ST + (2 * layer + 1) * 16384 + ((32 * half + hid) * 32 + lane) * 8, ln_pack(a, b), tagb + T_ST + 2 * layer + 1);
      }
      STAMP(4 + 8 * layer + 2 * half);
    }
    if (layer == 0) {
      const unsigned S = 32 * wv + hid;
      u32x4 g[7];
      poll_flags<SLH>(p.ws, WS_PS + S * 7168 + 4, lane == 0, tagb + T_PS, 1);
      #pragma unroll 1
      for (unsigned n = 0; n < SPIN_CAP; ++n) {
        RELOAD();
        bool ok = true;
        #pragma unroll
        for (int i = 0; i < 7; ++i) g[i] = __builtin_amdgcn_raw_buffer_load_b128(rws, lane * 16 + i * 1024, WS_PS + S * 7168, SC1);
        #pragma unroll
        for (int i = 0; i < 7; ++i) ok = ok && g[i].y == tagb + T_PS && g[i].w == tagb + T_PS;
        if (ok) break;
        __builtin_amdgcn_s_sleep(SLH);
      }
      STAMP(7);
      float a = 0.f, b = 0.f;
      #pragma unroll
      for (int i = 0; i < 7; ++i) { a += asf(g[i].x); b += asf(g[i].z); }
      a = sw32_add(a, b);
      a = sw16_add(a, a);
      b = asf(__builtin_amdgcn_permlane32_swap(__builtin_bit_cast(unsigned, a), __builtin_bit_cast(unsigned, a), false, false)[1]);
      if (lane < 16) st_gran(p.ws, WS_ST + 2 * 16384 + (S * 16 + lane) * 8, ln_pack(a, b), tagb + T_ST + 2);
      STAMP(8);
    }
    BAR();
  }
  STAMP_OUT();
}

constexpr int G2_ORD[14] = {0, 1, 2, 3, 9, 4, 10, 5, 11, 6, 12, 7, 13, 8};
struct Wts { u32x4 W1[56]; u32x4 WdA[8]; u32x4 WdV[28]; };

struct Rot { int a, b, st; DI int k(int o) const { return (o < 7 ? a : b) + (o % 7) * st; } };
DI void w1_piece(rsrc_t rw, unsigned vo, const unsigned (&gb)[4], unsigned m0w, int k0, int i) {
  const int slot = k0 + (i >> 1);
  asm volatile("s_mov_b32 m0, %0" :: "s"(m0w + i * 2048) : "memory");
  const unsigned so = gb[slot / 7] + ((slot % 7) * 16 + (i & 1) * 8) * F_ * 2;
  asm volatile("s_nop 0\n\tbuffer_load_dwordx4 %0, %1, %2 offen lds" :: "v"(vo), "s"(rw), "s"(so) : "memory");
}
template <int K0, int NK> DI void w1_read(Wts& w, lptr lds, unsigned rb) {
  #pragma unroll
  for (int s = 0; s < NK; ++s)
    #pragma unroll
    for (int t = 0; t < 2; ++t) {
      lptr a = lds + (rb ^ (t * 32)) + s * 4096;
      w.W1[2 * (K0 + s) + t] = cat(trr(a), trr(a + 1024));
    }
}
template <int K0, int NK> DI void w1_pin(Wts& w) {
  #pragma unroll
  for (int s = 2 * K0; s < 2 * (K0 + NK); ++s) asm volatile("" : "+a"(w.W1[s]));
}

DI void kt_build(lptr lds, int tid) {
  #pragma unroll
  for (int ch = 0; ch < 2; ++ch) {
    const u32x4 a = *(LAS u32x4*)(lds + LA_KC + ch * 4160 + tid * 16);
    const u32x2 b = *(LAS u32x2*)(lds + LA_KC + ch * 4160 + tid * 16 + 16);
    const unsigned e0 = __builtin_amdgcn_alignbyte(a.y, a.x, 2), e1 = __builtin_amdgcn_alignbyte(a.z, a.y, 2),
                   e2 = __builtin_amdgcn_alignbyte(a.w, a.z, 2), e3 = __builtin_amdgcn_alignbyte(b.x, a.w, 2),
                   e4 = __builtin_amdgcn_alignbyte(b.y, b.x, 2);
    lptr o = lds + LA_KT + ch * KT_CH + tid * 16;
    *(LAS u32x4*)(o) = a;
    *(LAS u32x4*)(o + KT_CP) = u32x4{e0, e1, e2, e3};
    *(LAS u32x4*)(o + 2 * KT_CP) = u32x4{a.y, a.z, a.w, b.x};
    *(LAS u32x4*)(o + 3 * KT_CP) = u32x4{e1, e2, e3, e4};
  }
  const u32x4 z = {0, 0, 0, 0};
  if (tid < 32) *(LAS u32x4*)(lds + LA_KT + (tid >> 2) * KT_CP + 4096 + (tid & 3) * 16) = z;
  if (tid < 248) { const int ch = tid >= 124, i = tid - 124 * ch; *(LAS u32x4*)(lds + LA_U + ch * U_CH + (i >> 2) * 80 + (i & 3) * 16) = z; }
}

DI u32x4 ld2x64(lptr p) { return cat(*(volatile LAS u32x2*)(p), *(volatile LAS u32x2*)(p + 8)); }
template <bool FIRST, class F> DI void conv_pass(f32x16& acc, lptr pa, lptr pb, F&& ride) {
  u32x4 A[4], B[4];
  auto off_a = [](int n) AI { return 32 * (n & 1) - 64 * (n >> 1) + 960; };
  auto off_b = [](int n) AI { return 32 * (n & 1) - 80 * (n >> 1) + 1200; };
  #pragma unroll
  for (int n = 0; n < 3; ++n) { A[n] = ld2x64(pa + off_a(n)); B[n] = *(LAS u32x4*)(pb + off_b(n)); }
  GAP();
  sfor<0, 32>([&](auto nc) AI {
    constexpr int n = decltype(nc)::value;
    if (FIRST && n == 0) asm volatile("v_mfma_f32_32x32x16_f16 %0, %1, %2, 0" : "=&v"(acc) : "v"(A[0]), "v"(B[0])); else mh32_vv(acc, A[n % 4], B[n % 4]);
    GAP();
    if (n + 3 < 32) { A[(n + 3) % 4] = ld2x64(pa + off_a(n + 3)); B[(n + 3) % 4] = *(LAS u32x4*)(pb + off_b(n + 3)); }
    GAP();
    ride(n);
    GAP();
  });
}

DI void x_fetch(rsrc_t rws, unsigned xvo, unsigned m0base, unsigned xso) {
  set_m0(m0base);
  dma<0, true>(xvo, rws, xso); dma<1024, true>(xvo, rws, xso); dma<2048, true>(xvo, rws, xso); dma<3072, true>(xvo, rws, xso);
  set_m0(m0base + 4096);
  dma<0, true>(xvo, rws, xso + 4096); dma<1024, true>(xvo, rws, xso + 4096); dma<2048, true>(xvo, rws, xso + 4096);
}
DI unsigned x_scan(lptr lds, unsigned xvo, unsigned m0base) {
  u32x4 v[7]; unsigned m = 0;
  #pragma unroll
  for (int i = 0; i < 7; ++i) v[i] = *(LAS u32x4*)(lds + m0base + xvo + i * 1024);
  #pragma unroll
  for (int i = 0; i < 7; ++i) m = umax4(m, v[i]);
  return m;
}
DI void x_retry(lptr lds, rsrc_t rws, unsigned xvo, unsigned m0base, unsigned xso) {
  #pragma unroll 1
  for (unsigned n = 0; n < RETRY_CAP; ++n) { x_fetch(rws, xvo, m0base, xso); VMWAIT(0); if (!any_empty(x_scan(lds, xvo, m0base))) break; }
}
template <int Q, class F>
DI void g1_step(const Wts& w, lptr lds, f32x16 (&cur)[2], f32x16 (&prev)[2], u32x4 (&B)[3], unsigned rd_cur, unsigned rd_next,
                float& c1, float& sd, u32x4 (&fix)[3], unsigned sttag, rsrc_t rws, unsigned hvo, unsigned hso,
                unsigned xvo, unsigned m0base, unsigned xso,
                unsigned stvo, unsigned stso, unsigned wso, F&& ride) {
  constexpr bool EPI = Q > 0, MMA = Q < 4, DMA = Q < 3;
  float e[16]; unsigned pk[8]; u32x4 xv[4]; unsigned xm = 0; u32x2 stn;
  auto gidx = [](int i) AI { return ((i >> 2) & 1) * 8 + (i & 3); };
  constexpr int NE = Q >= 3 ? 2 : 1, E0 = Q == 4 ? 16 : 3;
  int nx = 0, ns = 0;
  auto mean_fix = [&]() AI { mf32_vv(prev[0], fix[1], fix[0]); mf32_vv(prev[1], fix[2], fix[0]); };
  if (EPI && !MMA) { mean_fix(); asm volatile("s_nop 11" : "+v"(prev[0]), "+v"(prev[1])); }
  sfor<MMA ? 0 : E0, 28>([&](auto sc) AI {
    constexpr int s = decltype(sc)::value;
    if (MMA) {
      if (s == 0) mf32_first(cur[0], w.W1[0], B[0]); else mf32_acc(cur[0], w.W1[2 * s], B[s % 3]);
      GAP();
      const int sp = s + 2; lptr a = lds + (sp < 28 ? rd_cur + sp * 1024 : rd_next + (sp - 28) * 1024);
      if (sp < 28 || Q < 3) B[sp % 3] = cat(trr(a), trr(a + 256));
      GAP();
      if (EPI && s == 0) mean_fix();
      if (s == 23) { const u32x2 a0 = *(volatile LAS u32x2*)(lds + wso), a1 = *(volatile LAS u32x2*)(lds + wso + 264);
                     fix[1] = u32x4{a0.x, a0.y, 0, 0}; fix[2] = u32x4{a1.x, a1.y, 0, 0}; }
      if (s == 26) { sd = bfhi(stn.x); c1 = __builtin_amdgcn_rcpf(sd); const unsigned nm = ((stn.x & 0xffffu) ^ 0x8000u) * 0x10001u; fix[0] = u32x4{nm, nm, nm, nm};
                     asm volatile("" : "+v"(sd), "+v"(c1), "+v"(fix[0])); }
      if (s == 27) { c1 *= -1.4426950408889634f; asm volatile("" : "+v"(c1), "+v"(fix[1]), "+v"(fix[2])); }
    }
    constexpr int i = s - E0;
    auto grp = [&](auto gc, auto&& op) AI { constexpr int g = decltype(gc)::value; if constexpr (g >= 0 && g < 16 / NE) sfor<NE * g, NE * g + NE>(op); };
    if (EPI) {
      grp(std::integral_constant<int, i - 1>{}, [&](auto k) AI { e[k] = __builtin_amdgcn_exp2f(e[k]); });
      grp(std::integral_constant<int, i>{}, [&](auto k) AI { e[k] = prev[k >> 3][gidx(k)] * c1; });
      grp(std::integral_constant<int, i - 2>{}, [&](auto k) AI { e[k] = __builtin_fmaf(e[k], sd, sd); });
    }
    GAP();
    if (MMA) {
      if (s == 0) mf32_first(cur[1], w.W1[1], B[0]); else mf32_acc(cur[1], w.W1[2 * s + 1], B[s % 3]);
      GAP();
    }
    if (EPI) {
      grp(std::integral_constant<int, i - 2>{}, [&](auto k) AI { e[k] = __builtin_amdgcn_rcpf(e[k]); });
      grp(std::integral_constant<int, i - 3>{}, [&](auto k) AI { e[k] = prev[k >> 3][gidx(k) + 4] * e[k]; });
      grp(std::integral_constant<int, i - 4>{}, [&](auto k) AI { if constexpr (k & 1) pk[k >> 1] = pkbf_s(e[k - 1], e[k]); });
      if (i - 4 == 8 / NE) { __builtin_amdgcn_raw_buffer_store_b128(u32x4{pk[0], pk[1], pk[2], pk[3]}, rws, hvo, hso, SC1); ++nx; ++ns; }
      if (i - 4 == 16 / NE - 1) { GAP(); __builtin_amdgcn_raw_buffer_store_b128(u32x4{pk[4], pk[5], pk[6], pk[7]}, rws, hvo + 512, hso, SC1); ++nx; ++ns; }
    }
    GAP();
    constexpr int D0 = 0;
    if (DMA && s >= D0 && s < D0 + 7) {
      if (s == D0) { if (Q) asm volatile("s_barrier" ::: "memory");
                     set_m0(m0base); dma<0, true>(xvo, rws, xso); }
      if (s == D0 + 1) dma<1024, true>(xvo, rws, xso);
      if (s == D0 + 2) dma<2048, true>(xvo, rws, xso);
      if (s == D0 + 3) dma<3072, true>(xvo, rws, xso);
      if (s == D0 + 4) { set_m0(m0base + 4096); dma<0, true>(xvo, rws, xso + 4096); }
      if (s == D0 + 5) dma<1024, true>(xvo, rws, xso + 4096);
      if (s == D0 + 6) dma<2048, true>(xvo, rws, xso + 4096);
      ++ns; nx = 0;
    }
    if (MMA) {
      if (s == (Q == 0 ? 10 : 8)) { asm volatile("buffer_load_dwordx2 %0, %1, %2, %3 offen sc1" : "=v"(stn) : "v"(stvo), "s"(rws), "s"(stso) : "memory"); ++nx; ns = 0;
      }
      if (s == 24) {
        vmwait(ns); asm volatile("" : "+v"(stn));
        if (RARE(__builtin_amdgcn_ballot_w64(stn.y != sttag) != 0)) {
          #pragma unroll 1
          for (unsigned n = 0; n < SPIN_CAP; ++n) {
            asm volatile("buffer_load_dwordx2 %0, %1, %2, %3 offen sc1\n\ts_waitcnt vmcnt(0)" : "+v"(stn) : "v"(stvo), "s"(rws), "s"(stso) : "memory");
            if (__builtin_amdgcn_ballot_w64(stn.y != sttag) == 0) break;
          }
        }
      }
      if (DMA) {
        const lptr xp = lds + m0base + xvo;
        if (s == 20) vmwait(nx);
        if (s >= 22 && s < 25) { xm = umax4(xm, xv[2 * (s & 1)]); xm = umax4(xm, xv[2 * (s & 1) + 1]); }
        if (s >= 20 && s < 23) { xv[2 * (s & 1)] = *(LAS u32x4*)(xp + 2 * (s - 20) * 1024); xv[2 * (s & 1) + 1] = *(LAS u32x4*)(xp + (2 * (s - 20) + 1) * 1024); }
        if (s == 23) xv[2] = *(LAS u32x4*)(xp + 6 * 1024);
        if (s == 25) { if (RARE(any_empty(umax4(xm, xv[2])))) x_retry(lds, rws, xvo, m0base, xso); asm volatile("s_barrier" ::: "memory"); }
      }
    }
    GAP();
    { const int k = ride(s); nx += k; ns += k; }
    GAP();
  });
}

__global__ void __launch_bounds__(256, 1) fused_kernel(Params p) {
  extern __shared__ __attribute__((aligned(16))) unsigned char smem[];
  const lptr lds = (lptr)smem;
  const int tid = threadIdx.x, lane = tid & 63, wv = __builtin_amdgcn_readfirstlane(tid >> 6);
  const int b = blockIdx.x, xg = b & 7, idx = b >> 3;
  const bool worker = idx < 28;
  STAMP_DECL;
  STAMP(0);
  const rsrc_t rws = mk_rsrc(p.ws, WS_TOTAL);

  const unsigned epoch = ld_flag(p.ws, WS_EPOCH);
  const rsrc_t ru = mk_rsrc(p.u, ACT_BYTES);
  u32x4 xr[2];
  #pragma unroll
  for (int i = 0; i < 2; ++i) xr[i] = __builtin_amdgcn_raw_buffer_load_b128(ru, lane < 56 ? lane * 16 : 0xfffffff0u, (8 * b + 2 * wv + i) * 896, 0);
  const int c0 = worker ? 56 * xg + 2 * idx : 0;
  const int kch = c0 + (wv >> 1);
  unsigned pr[4];
  {
    const int o = (kch * N_ + (wv & 1) * 128) / 2 + lane;
    pr[0] = ((const unsigned*)p.lar)[o]; pr[1] = ((const unsigned*)p.aim)[o]; pr[2] = ((const unsigned*)p.cre)[o]; pr[3] = ((const unsigned*)p.cim)[o];
  }
  const unsigned ldt2 = *(const unsigned*)(p.ldt + c0), dsk2 = *(const unsigned*)(p.dsk + c0);
  const int sid = idx & 3;
  const int gam = 4 * (xg & 3) + sid, js = 7 * (xg >> 2) + (idx >> 2);
  const int s2 = 4 * xg + sid, cc = idx >> 2;
  const Rot rot = {14 * wv + ((sid & 1) ? 7 : 0) + ((sid & 2) ? 6 : 0), 14 * wv + ((sid & 1) ? 0 : 7) + ((sid & 2) ? 6 : 0), (sid & 2) ? -1 : 1};
  {
    unsigned long long pc; unsigned len;
    asm volatile("s_getpc_b64 %0\n.Lfused_code_here:\n\ts_mov_b32 %1, .Lfused_code_end-.Lfused_code_here" : "=s"(pc), "=s"(len));
    const rsrc_t rc = mk_rsrc((const void*)(pc & ~15ull), len & ~15u);
    set_m0(LA_DUMP);
    #pragma unroll
    for (int i = 0; i < 2; ++i) dma<0, false>(lane * 16, rc, (i * 32 + (idx & 7) * 4 + wv) * 1024);
  }
  auto mark = [&](int n) AI {
    const int t = fresh(threadIdx.x), l = t & 63;
    const u32x4 e4 = {EMPTY, EMPTY, EMPTY, EMPTY};
    if (n < 4) __builtin_amdgcn_raw_buffer_store_b128(e4, rws, worker ? ((t >> 3) * D_ + c0 + (t & 1)) * 64 + ((t >> 1) & 3) * 16 : 0xfffffff0u, WS_X1 + n * (ACT_BYTES / 2), SC1);
    else if (n < 8) __builtin_amdgcn_raw_buffer_store_b64(u32x2{EMPTY, EMPTY}, rws, (64 * cc + 16 * wv) * 32 + l * 8, WS_XT + ACT_BYTES + (32 * (n - 4) + s2) * D_ * 32, SC1);
    else __builtin_amdgcn_raw_buffer_store_b128(e4, rws, (((l >> 4) & 1) * 56 + 4 * js + wv) * 1024 + (16 * (l >> 5) + (l & 15)) * 16,
                                                WS_H + ((n - 8) >> 3) * H_BYTES + (2 * gam + 32 * (((n - 8) >> 1) & 3)) * 57344 + (n & 1) * 512, SC1);
  };
  const rsrc_t rw1 = mk_rsrc(wv < 2 ? p.wg : p.wu, D_ * F_ * 2);
  const unsigned w1vo = (4 * (wv & 1) + (lane >> 4)) * F_ * 2 + (((lane & 15) ^ ((2 * (lane >> 4) + (wv >> 1)) << 1)) * 16);
  const unsigned w1m0 = LA_W + (wv >> 1) * W_PL + (wv & 1) * 1024;
  unsigned w1gb[4];
  #pragma unroll
  for (int g = 0; g < 4; ++g) w1gb[g] = ((g + sid) & 3) * 112 * F_ * 2 + js * 256;
  unsigned kidx[8];
  #pragma unroll
  for (int j = 0; j < 8; ++j) {
    const int l = 2047 - 8 * tid - j;
    const unsigned pos = (unsigned)rbf((float)l), hb = pos >> 8;
    const unsigned sh = hb >= 4 ? 3u : (hb >= 2 ? 2u : hb);
    const unsigned base = (unsigned)((0x14ull << 40 | 0x13ull << 35 | 0x12ull << 30 | 0x11ull << 25 | 0x10ull << 20 | 0xeull << 15 | 0xcull << 10 | 0x8ull << 5) >> (5 * hb)) & 31u;
    kidx[j] = ((base + ((pos & 7u) >> sh)) * 36 + ((pos & 255u) >> 3)) * 4;
    asm volatile("" : "+v"(kidx[j]));
  }
  GAP();
  const unsigned ep = __builtin_amdgcn_readfirstlane(epoch), tagb = (ep + 1u) * 0x9E3779B1u;

  unsigned st1;
  {
    float s[2], q[2];
    asm volatile("" : "+v"(xr[0]), "+v"(xr[1]), "+v"(pr[0]), "+v"(pr[1]), "+v"(pr[2]), "+v"(pr[3]));
    #pragma unroll
    for (int i = 0; i < 2; ++i) {
      f32x2 a = {0.f, 0.f}, c = {0.f, 0.f};
      #pragma unroll
      for (int d = 0; d < 4; ++d) { f32x2 x = {bflo(xr[i][d]), bfhi(xr[i][d])}; a += x; c = __builtin_elementwise_fma(x, x, c); }
      s[i] = a.x + a.y; q[i] = c.x + c.y;
      if (lane < 56) *(LAS u32x4*)(lds + LA_KC + (2 * wv + i) * 896 + lane * 16) = xr[i];
    }
    float r = row_sum(sw16_add(sw32_add(s[0], q[0]), sw32_add(s[1], q[1])));
    const float o = asf(__builtin_amdgcn_permlane32_swap(__builtin_bit_cast(unsigned, r), __builtin_bit_cast(unsigned, r), false, false)[1]);
    st1 = ln_pack(r, o);
    BAR();
    #pragma unroll
    for (int i = 0; i < 2; ++i) {
      const int g = 4 * i + (lane >> 4), cg = wv * 7 + (g < 7 ? g : 0);
      const lptr a = lds + LA_KC + ((lane >> 2) & 3) * 896 + cg * 32 + (lane & 3) * 8;
      __builtin_amdgcn_raw_buffer_store_b128(cat(trr(a), trr(a + 4 * 896)), rws, g < 7 ? (cg * 16 + (lane & 15)) * 16 : 0xfffffff0u, WS_XT + b * D_ * 16, SC1);
    }
  }
  mark(0); mark(1);
  const unsigned st1off = WS_ST + (8 * b + 2 * wv + (lane >> 4)) * 8;
  if (__builtin_expect(worker, 1)) {
  STAMP(1);

  Wts w;
  const unsigned w1rb = LA_W + ((lane >> 1) & 1) * W_PL + (8 * (lane >> 5) + ((lane >> 2) & 3)) * 256 +
                        (((4 * wv + ((lane >> 4) & 1)) ^ ((2 * ((lane >> 2) & 3) + ((lane >> 1) & 1)) << 1)) * 16) + (lane & 1) * 8;

  const float dk = (tid & 1) ? bfhi(dsk2) : bflo(dsk2);
  u32x4 gr[2][4], xa[2], uh[2]; float base[2][8];
  auto fetch = [&](const int layer, const int half) AI {
    const int t = fresh(threadIdx.x);
    #pragma unroll
    for (int i = 0; i < 4; ++i) gr[half][i] = __builtin_amdgcn_raw_buffer_load_b128(rws, (t >> 1) * 64 + i * 16, WS_ST + 2 * layer * 16384 + half * 8192, SC1);
    GAP();
  };
  auto valid = [&](const int layer, const int half) AI {
    const unsigned tag = tagb + T_ST + 2 * layer;
    bool ok = true;
    #pragma unroll
    for (int i = 0; i < 4; ++i) ok = ok && gr[half][i].y == tag && gr[half][i].w == tag;
    return ok;
  };
  auto xload = [&](const int layer, const int half) AI {
    const int t = fresh(threadIdx.x);
    const unsigned vo = layer ? ((t >> 2) * D_ + c0 + (t & 1)) * 32 + (t & 2) * 8 : ((t >> 1) * D_ + c0 + (t & 1)) * 16;
    xa[half] = __builtin_amdgcn_raw_buffer_load_b128(rws, vo, WS_XT + layer * ACT_BYTES + half * (ACT_BYTES / 2), SC1);
    GAP();
  };
  auto settle = [&](const int layer, const int half) AI {
    if (RARE(!__all(valid(layer, half) && (layer == 0 || umax4(0, xa[half]) != EMPTY)))) {
      #pragma unroll 1
      for (unsigned n = 0; n < SPIN_CAP; ++n) {
        RELOAD(); if (layer) xload(layer, half);
        fetch(layer, half);
        if (valid(layer, half) && (layer == 0 || n >= RETRY_CAP || umax4(0, xa[half]) != EMPTY)) break;
      }
      VMCLEAN();
    }
  };
  auto normalise = [&](const int half) AI {
    float u[8];
    #pragma unroll
    for (int j = 0; j < 8; ++j) {
      const unsigned v = (j & 1) ? gr[half][j >> 1].z : gr[half][j >> 1].x, wd = xa[half][j >> 1];
      const float x = (j & 1) ? bfhi(wd) : bflo(wd);
      u[j] = (x - bflo(v)) * __builtin_amdgcn_rcpf(bfhi(v));
      base[half][j] = __builtin_fmaf(u[j], dk, x);
    }
    uh[half] = u32x4{pkh(u[0], u[1]), pkh(u[2], u[3]), pkh(u[4], u[5]), pkh(u[6], u[7])};
  };
  auto u_write = [&](const int half) AI {
    const int t = fresh(threadIdx.x);
    *(LAS u32x4*)(lds + LA_U + (t & 1) * U_CH + ((t >> 3) + 32 * half + 31) * 80 + ((t >> 1) & 3) * 16) = uh[half];
  };

  const float dt0 = rbf(expf(bflo(ldt2))), dt1 = rbf(expf(bfhi(ldt2)));
  float ysc;
  {
    const float dt = (wv >> 1) ? dt1 : dt0;
    unsigned mx = max(max(pr[2] & 0x7fffu, pr[3] & 0x7fffu), max((pr[2] >> 16) & 0x7fffu, (pr[3] >> 16) & 0x7fffu));
    mx = dpp_umax<DPP_ROR8>(dpp_umax<DPP_ROR4>(dpp_umax<DPP_X2>(dpp_umax<DPP_X1>(mx))));
    unsigned eb = max(max(__builtin_amdgcn_readlane(mx, 0), __builtin_amdgcn_readlane(mx, 16)), max(__builtin_amdgcn_readlane(mx, 32), __builtin_amdgcn_readlane(mx, 48))) >> 7;
    eb = min(max(eb, 1u), 253u);
    const float cs = __builtin_bit_cast(float, (254u - eb) << 23);
    const lptr tb = lds + wv * TB_W;
    STAMP(2);
    auto ride = [&](int n) AI { w1_piece(rw1, w1vo, w1gb, w1m0, 0, n); GAP(); };
    f32x2 z1[2], z1r[2], zb[2], zbr[2], z8[2], zs[2], zsr[2], v[2][21];
    #pragma unroll
    for (int m = 0; m < 2; ++m) {
      auto part = [&](unsigned x) AI { return m ? bfhi(x) : bflo(x); };
      const float lr = -rbf(expf(part(pr[0]))) * dt * 1.4426950408889634f;
      const float li = part(pr[1]) * dt * 0.15915494309189535f;
      auto zpow = [&](float pw) AI { const float a = __builtin_amdgcn_exp2f(lr * pw); float t = li * pw; t -= __builtin_rintf(t);
                                     return f32x2{a * __builtin_amdgcn_cosf(t), a * __builtin_amdgcn_sinf(t)}; };
      const f32x2 y1 = zpow(1.f), yb = zpow(256.f);
      z8[m] = zpow(8.f); zs[m] = zpow(16.f); zsr[m] = f32x2{-zs[m].y, zs[m].x};
      z1[m] = f32x2{y1.x, -y1.y}; z1r[m] = f32x2{y1.y, y1.x};
      zb[m] = f32x2{yb.x, -yb.y}; zbr[m] = f32x2{yb.y, yb.x};
      v[m][0] = f32x2{part(pr[2]) * cs, -part(pr[3]) * cs};
    }
    GAP();
    volatile LAS u32x2* o = (volatile LAS u32x2*)(tb + lane * 8);
    #pragma unroll
    for (int r = 0; r < 21; ++r) {
      const int c = r + 1, sc = c < 8 ? c - 1 : c < 12 ? 2 * (c - 8) : c == 12 ? 8 : c == 13 ? 10 : c < 17 ? c - 2 : c - 1;
      f32x2 t0, t1;
      if (c < 21) { t0 = v[0][sc].xx * (c < 8 ? z1[0] : zb[0]); GAP(); t1 = v[1][sc].xx * (c < 8 ? z1[1] : zb[1]); GAP(); }
      const unsigned p0 = pkh(v[0][r].x, v[0][r].y); GAP();
      const unsigned p1 = pkh(v[1][r].x, v[1][r].y); GAP();
      if (c < 21) { v[0][c] = __builtin_elementwise_fma(v[0][sc].yy, c < 8 ? z1r[0] : zbr[0], t0); GAP();
                    v[1][c] = __builtin_elementwise_fma(v[1][sc].yy, c < 8 ? z1r[1] : zbr[1], t1); GAP(); }
      o[r * 66] = u32x2{p0, p1}; GAP();
      if (r % 2 == 0) ride(r / 2);
      if (r == 9) {
        asm volatile("s_waitcnt vmcnt(5)\n\ts_barrier" ::: "memory");
        __builtin_amdgcn_raw_buffer_store_b64(u32x2{st1, tagb + T_ST}, rws, (lane & 47) == 0 ? st1off : 0xfffffff0u, 0, SC1);
      }
    }
    STAMP(3);
    u32x4 fb[16];
    #pragma unroll
    for (int s = 0; s < 16; ++s) fb[s] = *(LAS u32x4*)(tb + min(lane & 31, 20) * 528 + (lane >> 5) * 16 + s * 32);
    RELOAD();
    f32x2 x[4] = {f32x2{1.f, 0.f}, f32x2{1.f, 0.f}, z8[0], z8[1]};
    #pragma unroll
    for (int i = 0; i < 16; ++i) {
      f32x2 t[4]; unsigned pk[4];
      #pragma unroll
      for (int u = 0; u < 4; ++u) { pk[u] = pkh(x[u].x, x[u].y); GAP(); }
      if (i < 15) {
        #pragma unroll
        for (int u = 0; u < 4; ++u) { t[u] = x[u].xx * zs[u & 1]; GAP(); }
      }
      o[(2 * i) * 66] = u32x2{pk[0], pk[1]}; GAP();
      if (i < 15) {
        #pragma unroll
        for (int u = 0; u < 4; ++u) { x[u] = __builtin_elementwise_fma(x[u].yy, zsr[u & 1], t[u]); GAP(); }
      }
      o[(2 * i + 1) * 66] = u32x2{pk[2], pk[3]}; GAP();
      if (i < 9) ride(11 + i);
    }
    STAMP(4);
    fetch(0, 0); fetch(0, 1);
    f32x16 acc;
    {
      u32x4 fa[16];
      #pragma unroll
      for (int s = 0; s < 16; ++s) fa[s] = *(LAS u32x4*)(tb + (lane & 31) * 528 + (lane >> 5) * 16 + s * 32);
      asm volatile("v_mfma_f32_32x32x16_f16 %0, %1, %2, 0" : "=&v"(acc) : "v"(fa[0]), "v"(fb[0]));
      #pragma unroll
      for (int s = 1; s < 16; ++s) mh32_vv(acc, fa[s], fb[s]);
    }
    asm volatile("s_nop 10\n\ts_barrier" : "+v"(acc) :: "memory");
    if ((lane & 31) < 21) {
      #pragma unroll
      for (int i = 0; i < 4; ++i)
        *(LAS f32x4*)(lds + LA_RED + ((wv * 21 + (lane & 31)) * 36 + 8 * i + 4 * (lane >> 5)) * 4) = f32x4{acc[4 * i], acc[4 * i + 1], acc[4 * i + 2], acc[4 * i + 3]};
    }
    if (lane == 0) *(LAS unsigned*)(lds + LA_MISC + wv * 4) = eb;
    settle(0, 0); settle(0, 1);
    VMCLEAN();
    xload(0, 0); xload(0, 1);
    BAR();
    STAMP(5);
    st_flag_if(rws, b == 0 && tid == 0, WS_EPOCH, ep + 1u);
    w1_read<0, 10>(w, lds, w1rb);
    GAP();
    const u32x4 e4 = *(LAS u32x4*)(lds + LA_MISC);
    #pragma unroll
    for (int ch = 0; ch < 2; ++ch) {
      const unsigned ea = e4[2 * ch], ebb = e4[2 * ch + 1], em = max(ea, ebb);
      const float fa = em - ea > 120u ? 0.f : __builtin_bit_cast(float, (132u - (em - ea)) << 23);
      const float fb = em - ebb > 120u ? 0.f : __builtin_bit_cast(float, (132u - (em - ebb)) << 23);
      float k[8];
      #pragma unroll
      for (int j = 0; j < 8; ++j)
        k[j] = *(LAS float*)(lds + LA_RED + (2 * ch) * 3024 + kidx[j]) * fa + *(LAS float*)(lds + LA_RED + (2 * ch + 1) * 3024 + kidx[j]) * fb;
      *(LAS u32x4*)(lds + LA_KC + ch * 4160 + tid * 16) = u32x4{pkh(k[0], k[1]), pkh(k[2], k[3]), pkh(k[4], k[5]), pkh(k[6], k[7])};
      const float ys = (ch ? dt1 : dt0) * __builtin_bit_cast(float, em << 23) * 0.0625f;
      if (ch == 0 || (tid & 1)) ysc = ys;
    }
    if (tid < 8) *(LAS u32x4*)(lds + LA_KC + (tid >> 2) * 4160 + 4096 + (tid & 3) * 16) = u32x4{0, 0, 0, 0};
    w1_pin<0, 10>(w);
    normalise(0); normalise(1);
    BAR();
  }
  STAMP(6);

  constexpr unsigned xq = 16 * XB_SZ, hq = 32 * 57344;
  unsigned wvx = wv * 7168, xsrc = gam * XB_SZ + ((wv + sid) & 3) * 7168;
  asm volatile("" : "+s"(wvx), "+s"(xsrc));
  auto conv_out = [&](const int layer, const int half, f32x16& acc, auto&& after_bar) AI {
    const int tid = fresh(threadIdx.x), lane = tid & 63;
    asm volatile("s_nop 11" : "+v"(acc));
    lptr py = lds + LA_Y + ((wv & 1) * 2 + (wv >> 1)) * 4608 + (lane & 31) * 144 + 16 * (lane >> 5);
    #pragma unroll
    for (int i = 0; i < 4; ++i) *(LAS f32x4*)(py + 32 * i) = f32x4{acc[4 * i], acc[4 * i + 1], acc[4 * i + 2], acc[4 * i + 3]};
    BAR();
    after_bar();
    const lptr q = lds + LA_Y + (tid & 1) * 4608 + (tid >> 3) * 144 + ((tid >> 1) & 3) * 32;
    const f32x4 y0 = *(LAS f32x4*)(q) + *(LAS f32x4*)(q + 9216), y1 = *(LAS f32x4*)(q + 16) + *(LAS f32x4*)(q + 9232);
    const float* bs = base[half];
    const u32x4 v = {pkbf(__builtin_fmaf(y0[0], ysc, bs[0]), __builtin_fmaf(y0[1], ysc, bs[1])), pkbf(__builtin_fmaf(y0[2], ysc, bs[2]), __builtin_fmaf(y0[3], ysc, bs[3])),
                     pkbf(__builtin_fmaf(y1[0], ysc, bs[4]), __builtin_fmaf(y1[1], ysc, bs[5])), pkbf(__builtin_fmaf(y1[2], ysc, bs[6]), __builtin_fmaf(y1[3], ysc, bs[7]))};
    __builtin_amdgcn_raw_buffer_store_b128(v, rws, ((tid >> 3) * D_ + c0 + (tid & 1)) * 64 + ((tid >> 1) & 3) * 16, WS_X1 + layer * ACT_BYTES + half * 32 * D_ * 64, SC1);
  };
  auto conv = [&](auto layer_c) AI {
    constexpr int layer = decltype(layer_c)::value;
    const int lane = fresh(threadIdx.x) & 63;
    if (layer) {
      xload(1, 0); fetch(1, 0);
      kt_build(lds, fresh(threadIdx.x));
      STAMP(7 + 24 * layer);
      settle(1, 0);
      STAMP(8 + 24 * layer);
      normalise(0);
    } else {
      #pragma unroll
      for (int n = 0; n < 4; ++n) w1_piece(rw1, w1vo, w1gb, w1m0, 10, n);
      kt_build(lds, fresh(threadIdx.x));
    }
    u_write(0);
    if (layer == 0) u_write(1);
    BAR();
    STAMP(10 + 24 * layer);
    const int ch = wv >> 1, d1 = 16 * (wv & 1) + 15;
    const lptr pa = lds + LA_KT + ch * KT_CH + (3 - (lane & 3)) * KT_CP + 4088 - 8 * ((lane & 31) >> 2) + 16 * (lane >> 5) - 64 * d1;
    const lptr pb = lds + LA_U + ch * U_CH + ((lane & 31) - d1 + 31) * 80 + 16 * (lane >> 5);
    f32x16 acc;
    conv_pass<true>(acc, pa, pb, [&](int n) AI { if (layer == 0 && n % 4 == 0) w1_piece(rw1, w1vo, w1gb, w1m0, 10, 4 + n / 4);
                                                 if (layer == 0 ? n % 4 == 2 : n % 8 == 3) mark(layer ? 16 + n / 8 : 8 + n / 4); });
    STAMP(11 + 24 * layer);
    conv_out(layer, 0, acc, []() AI {});
    STAMP(12 + 24 * layer);
    conv_pass<true>(acc, pa - 2048, pb, [&](int n) AI {
      if (layer == 0 && n < 16 && !(n & 1)) w1_piece(rw1, w1vo, w1gb, w1m0, 10, 12 + n / 2);
      if (layer == 0 && n < 12 && (n & 1)) mark(2 + n / 2);
      if (layer == 1 && n == 4) { xload(1, 1); fetch(1, 1); }
      if (layer == 1 && n >= 8 && n % 6 == 2) mark(20 + (n - 8) / 6);
    });
    if (layer == 0) {
      VMWAIT(0); asm volatile("s_barrier" ::: "memory");
      w1_read<10, 10>(w, lds, w1rb);
      w1_pin<10, 10>(w);
    } else {
      settle(1, 1);
      normalise(1); u_write(1);
    }
    BAR();
    STAMP(13 + 24 * layer);
    const unsigned csvo = (((lane >> 3) & 1) * F_ + 128 * js + 32 * wv + 8 * ((lane >> 4) & 1) + (lane & 7)) * 8;
    unsigned fw; u32x2 cs[2];
    auto cs_load = [&]() AI {
      #pragma unroll
      for (int t = 0; t < 2; ++t) cs[t] = __builtin_amdgcn_raw_buffer_load_b64(rws, csvo + t * 128, WS_CS, SC1);
      fw = __builtin_amdgcn_raw_buffer_load_b32(rws, (lane & 31) * 64, WS_FW, SC1);
    };
    conv_pass<false>(acc, pa, pb + 2560, [&](int n) AI {
      if (layer == 0 && n < 23 && n % 3 != 2) w1_piece(rw1, w1vo, w1gb, w1m0, 20, n - n / 3);
      if (layer == 0 && n == 24) cs_load();
    });
    STAMP(9 + 24 * layer);
    const unsigned xso = WS_X1 + layer * ACT_BYTES + xsrc;
    if (layer == 1) VMWAIT(0);
    if (layer == 0) { asm volatile("" : "+v"(cs[0]), "+v"(cs[1]), "+v"(fw)); }
    conv_out(layer, 1, acc, [&]() AI { x_fetch(rws, lane * 16, wvx, xso); });
    if (layer == 0) w1_read<20, 8>(w, lds, w1rb);
    if (layer == 0) {
      if (RARE(!__all(cs[0].y == tagb + T_CS && cs[1].y == tagb + T_CS && fw == tagb + T_FW))) {
        #pragma unroll 1
        for (unsigned n = 0; n < SPIN_CAP; ++n) { RELOAD(); cs_load(); if (cs[0].y == tagb + T_CS && cs[1].y == tagb + T_CS && fw == tagb + T_FW) break; }
        VMCLEAN();
      }
      #pragma unroll
      for (int t = 0; t < 2; ++t) {
        const float a = asf(cs[t].x), h = rbf(a), m = rbf(a - h), l = a - h - m;
        if (lane < 33) *(LAS u32x2*)(lds + LA_DUMP + wv * 528 + t * 264 + lane * 8) = lane < 32 ? u32x2{pkbf(h, m), pkbf(l, 0.f)} : u32x2{0, 0};
      }
    }
    if (layer == 0) w1_pin<20, 8>(w);
    STAMP(14 + 24 * layer);
    VMWAIT(1);
    if (RARE(any_empty(x_scan(lds, lane * 16, wvx)))) x_retry(lds, rws, lane * 16, wvx, xso);
    BAR();
    STAMP(16 + 24 * layer);
  };

  auto ffn = [&](auto layer_c) AI {
    constexpr int layer = decltype(layer_c)::value;
    const int lane = fresh(threadIdx.x) & 63;
    const unsigned hb = WS_H + layer * H_BYTES + s2 * 57344;
    u32x4 Bh[14]; unsigned ko[14];
    #pragma unroll
    for (int u = 0; u < 14; ++u) { ko[u] = rot.k(G2_ORD[u]) * 1024; asm volatile("" : "+s"(ko[u])); }
    {
      f32x16 A0[2], A1[2];
      const unsigned xso = WS_X1 + layer * ACT_BYTES + xsrc;
      const unsigned hvo = (((lane >> 4) & 1) * 56 + 4 * js + wv) * 1024 + (16 * (lane >> 5) + (lane & 15)) * 16;
      const unsigned hso = WS_H + layer * H_BYTES + 2 * gam * 57344;
      const unsigned stvo = (32 * gam + (lane & 31)) * 8, stso = WS_ST + (2 * layer + 1) * 16384, sttag = tagb + T_ST + 2 * layer + 1;
      const unsigned rb = (8 * (lane >> 5) + ((lane >> 2) & 3)) * 64 + ((lane >> 4) & 1) * 32 + (lane & 3) * 8;
      const unsigned wso = LA_DUMP + wv * 528 + min(lane, 32) * 8;
      u32x4 B[3], fix[3]; float c1, sd;
      auto wd_ride = [&](int n) AI {
        if (n < 20) {
          const unsigned so = WS_WD + (cc * 56 + rot.k(9 + (n >> 2))) * 4096;
          if ((n & 3) == 0) set_m0(LA_W + wv * 20480 + (n >> 2) * 4096);
          if ((n & 3) == 0) dma<0, true>(lane * 16, rws, so);
          if ((n & 3) == 1) dma<1024, true>(lane * 16, rws, so);
          if ((n & 3) == 2) dma<2048, true>(lane * 16, rws, so);
          if ((n & 3) == 3) dma<3072, true>(lane * 16, rws, so);
        } else {
          const int m = n - 20;
          const u32x4 f = __builtin_amdgcn_raw_buffer_load_b128(rws, lane * 16 + (m & 3) * 1024, WS_WD + (cc * 56 + rot.k(m >> 2)) * 4096, SC1);
          if (m < 8) w.WdA[m] = f; else w.WdV[m - 8] = f;
        }
        return 1;
      };
      auto h_first = [&](int n) AI { Bh[G2_ORD[n]] = __builtin_amdgcn_raw_buffer_load_b128(rws, lane * 16, hb + ko[n], SC1); return 1; };
      STAMP(17 + 24 * layer);
      #pragma unroll
      for (int s = 0; s < 2; ++s) B[s] = cat(trr(lds + rb + s * 1024), trr(lds + rb + s * 1024 + 256));
      g1_step<0>(w, lds, A0, A1, B, rb, rb + XB_SZ, c1, sd, fix, sttag, rws, hvo, hso, lane * 16, XB_SZ + wvx, xso + xq, stvo, stso, wso,
                 [&](int s) AI { return layer == 0 && s >= 7 && s < 23 && (s & 1) ? wd_ride((s - 7) >> 1) : 0; });
      { u32x4 t = B[0]; B[0] = B[1]; B[1] = B[2]; B[2] = t; }
      STAMP(18 + 24 * layer);
      g1_step<1>(w, lds, A1, A0, B, rb + XB_SZ, rb, c1, sd, fix, sttag, rws, hvo, hso, lane * 16, wvx, xso + 2 * xq, stvo, stso + 4096, wso,
                 [&](int s) AI { return layer == 0 && s >= 7 && s < 23 ? wd_ride(8 + s - 7) : 0; });
      { u32x4 t = B[0]; B[0] = B[1]; B[1] = B[2]; B[2] = t; }
      STAMP(19 + 24 * layer);
      g1_step<2>(w, lds, A0, A1, B, rb, rb + XB_SZ, c1, sd, fix, sttag, rws, hvo, hso + hq, lane * 16, XB_SZ + wvx, xso + 3 * xq, stvo, stso + 8192, wso,
                 [&](int s) AI { return layer == 0 && s >= 7 && s < 23 ? wd_ride(24 + s - 7) : 0; });
      { u32x4 t = B[0]; B[0] = B[1]; B[1] = B[2]; B[2] = t; }
      STAMP(20 + 24 * layer);
      g1_step<3>(w, lds, A1, A0, B, rb + XB_SZ, rb, c1, sd, fix, sttag, rws, hvo, hso + 2 * hq, lane * 16, 0, 0, stvo, stso + 12288, wso,
                 [&](int s) AI { if (s >= 17 && s < 27) return h_first(s - 17);
                                 return layer == 0 && s >= 1 && s < 17 ? wd_ride(39 + s) : 0; });
      STAMP(21 + 24 * layer);
      g1_step<4>(w, lds, A0, A1, B, 0, 0, c1, sd, fix, sttag, rws, hvo, hso + 3 * hq, 0, 0, 0, 0, 0, wso,
                 [&](int s) AI { return s >= 21 && s < 25 ? h_first(s - 11) : 0; });
    }
    STAMP(22 + 24 * layer);
    const int ech = layer ? lane : 16 * wv + (lane >> 2), etq = layer ? wv : lane & 3;
    constexpr unsigned PW = layer ? PT_W : PT_W0, PP = layer ? 20 : 16;
    const unsigned rvo = ((s2 >> 1) * D_ + 64 * cc + ech) * 64 + ((s2 & 1) * 16 + 4 * etq) * 2;
    const lptr wl = lds + LA_W + wv * 20480 + lane * 16;
    if (layer == 0) {
      #pragma unroll
      for (int i = 0; i < 8; ++i) asm volatile("" : "+a"(w.WdA[i]));
    }
    asm volatile("s_barrier" ::: "memory");
    STAMP(23 + 24 * layer);
    u32x2 res; f32x4 y[4], aa[8]; unsigned o0, o1; float x[4], r[4], t0, t1; u32x2 sw[4];
    const rsrc_t rout = mk_rsrc(p.out, ACT_BYTES);
    const lptr pwr = lds + wv * PW + ((32 * ((lane >> 4) & 1) + 4 * (lane >> 5)) * PP + (lane & 15)) * 4, prd = lds + ech * PP * 4 + etq * 16;
    const unsigned sto = layer ? (4 * wv * D_ + 64 * cc + lane) * 2 : (64 * cc + 16 * wv) * 32 + lane * 8;
    const unsigned gso = (lane & 8) == 0 ? ((4 * (lane & 3) + 2 * ((lane >> 2) & 1) + ((lane >> 4) & 1)) * 2 + (lane >> 5)) * 8 : 0xfffffff0u;
    auto sw32 = [&](float a, float b) AI { auto z = __builtin_amdgcn_permlane32_swap(__builtin_bit_cast(unsigned, a), __builtin_bit_cast(unsigned, b), false, false); return u32x2{z[0], z[1]}; };
    auto sw16 = [&](float a, float b) AI { auto z = __builtin_amdgcn_permlane16_swap(__builtin_bit_cast(unsigned, a), __builtin_bit_cast(unsigned, b), false, false); return u32x2{z[0], z[1]}; };
    constexpr int NOP = layer ? 49 : 73;
    auto epi = [&](int q, int k) AI {
      const unsigned S = 32 * q + s2;
      if (k < 8) { LAS float* o = (LAS float*)(pwr + (q & 1) * PT_SZ) + (8 * (k & 3) + 2 * (k >> 2)) * PP;
                   o[0] = aa[4 * (q & 1) + 2 * ((k >> 2) & 1)][k & 3]; o[PP] = aa[4 * (q & 1) + 2 * ((k >> 2) & 1) + 1][k & 3]; }
      if (k == 8) BAR();
      if (k >= 9 && k < 13) y[k - 9] = *(LAS f32x4*)(prd + (q & 1) * PT_SZ + (k - 9) * PW);
      if (k >= 13 && k < 17) x[k - 13] = (k & 1) ? bflo(k < 15 ? res.x : res.y) : bfhi(k < 15 ? res.x : res.y);
      if (k >= 22 && k < 26) r[k - 22] = fadd_s(y[0][k - 22], y[1][k - 22]);
      if (k >= 26 && k < 30) y[2][k - 26] = fadd_s(y[2][k - 26], y[3][k - 26]);
      if (k >= 30 && k < 34) r[k - 30] = fadd_s(r[k - 30], x[k - 30]);
      if (k >= 34 && k < 38) r[k - 34] = fadd_s(r[k - 34], y[2][k - 34]);
      if (k == 38) o0 = pkbf_s(r[0], r[1]);
      if (k == 39) o1 = pkbf_s(r[2], r[3]);
      if (layer == 0) {
        if (k == 41) { __builtin_amdgcn_raw_buffer_store_b64(u32x2{o0, o1}, rws, sto, WS_XT + ACT_BYTES + S * D_ * 32, SC1); }
        if (k >= 42 && k < 46) x[k - 42] = (k & 1) ? bfhi(k < 44 ? o0 : o1) : bflo(k < 44 ? o0 : o1);
        if (k >= 46 && k < 50) { r[k - 46] = x[k - 46] * x[k - 46]; asm volatile("" : "+v"(r[k - 46])); }
        if (k >= 50 && k < 54) sw[k - 50] = sw32(x[k - 50], r[k - 50]);
        if (k >= 54 && k < 58) r[k - 54] = fadd_s(asf(sw[k - 54].x), asf(sw[k - 54].y));
        if (k == 60) sw[0] = sw16(r[0], r[1]);
        if (k == 61) sw[1] = sw16(r[2], r[3]);
        if (k == 62) t0 = fadd_s(asf(sw[0].x), asf(sw[0].y));
        if (k == 63) t1 = fadd_s(asf(sw[1].x), asf(sw[1].y));
        if (k == 66) t0 = dpp_add<DPP_ROR4>(t0);
        if (k == 67) t1 = dpp_add<DPP_ROR4>(t1);
        if (k == 70) t0 = dpp_add<DPP_ROR8>(t0);
        if (k == 71) t1 = dpp_add<DPP_ROR8>(t1);
        if (k == 72) {
          __builtin_amdgcn_raw_buffer_store_b64(u32x2{__builtin_bit_cast(unsigned, (lane & 4) ? t1 : t0), tagb + T_PS}, rws, gso, WS_PS + (S * 28 + 4 * cc + wv) * 256, SC1);
        }
      } else if (k >= 42 && !(k & 1)) {
        const int j = (k - 42) >> 1; const unsigned v = j < 2 ? o0 : o1;
        __builtin_amdgcn_raw_buffer_store_b16((u16)((j & 1) ? v >> 16 : v), rout, sto + j * D_ * 2, 16 * S * D_ * 2, 0);
      }
      GAP();
    };
    u32x4 wf[4];
    #pragma unroll
    for (int t = 0; t < 4; ++t) wf[t] = *(LAS u32x4*)(wl + t * 1024);
    sfor<0, 4>([&](auto qc) AI {
      constexpr int q = decltype(qc)::value;
      unsigned hm = 0;
      auto settle_h = [&](int lo, int hi) AI {
        if (RARE(any_empty(hm))) {
          unsigned hc = hb; asm volatile("" : "+s"(hc));
          #pragma unroll 1
          for (unsigned n = 0; n < RETRY_CAP; ++n) {
            #pragma unroll
            for (int u = lo; u < hi; ++u) reload128(Bh[G2_ORD[u]], rws, lane * 16, hc + q * hq + ko[u]);
            VMWAIT(0);
            hm = 0;
            #pragma unroll
            for (int u = lo; u < hi; ++u) { asm volatile("" : "+v"(Bh[G2_ORD[u]])); hm = umax4(hm, Bh[G2_ORD[u]]); }
            if (!any_empty(hm)) break;
          }
        }
        GAP();
      };
      #pragma unroll
      for (int u = 0; u < 4; ++u) hm = umax4(hm, Bh[G2_ORD[u]]);
      settle_h(0, 4);
      const rsrc_t rh = mk_rsrc(p.ws + hb + (q + 1) * hq, 57344);
      sfor<0, 56>([&](auto gc) AI {
        constexpr int g = decltype(gc)::value, s = g >> 2, t = g & 3, o = G2_ORD[s];
        constexpr int ai = 4 * (q & 1) + t;
        if constexpr (o == 0) mf16_a0(aa[ai], w.WdA[t], Bh[0]);
        else if constexpr (o == 1) mf16_a(aa[ai], w.WdA[4 + t], Bh[1]);
        else if constexpr (o < 9) mf16_v(aa[ai], w.WdV[4 * (o - 2) + t], Bh[o]);
        else mf16_v(aa[ai], wf[t], Bh[o]);
        GAP();
        if (o >= 9 && (o < 13 || q < 3)) { wf[t] = *(LAS u32x4*)(wl + (4 * ((o - 8) % 5) + t) * 1024); GAP(); }
        {
          constexpr int u = g >= 10 && g < 15 ? 4 + g - 10 : g >= 27 && g < 36 && (g & 1) ? 9 + (g - 27) / 2 : -1;
          if (u >= 0) { hm = umax4(hm, Bh[G2_ORD[u < 0 ? 0 : u]]); asm volatile("" : "+v"(hm)); GAP(); }
        }
        if (q > 0 && g >= 3) {
          constexpr int R = layer == 0 && q == 2 ? 3 : 2;
          constexpr int k0 = g < 11 ? g - 3 : g < 13 ? NOP : g == 13 ? 8 : g < 20 ? 9 + 2 * (g - 14) : 21 + R * (g - 20), k1 = g < 14 ? k0 + 1 : g < 20 ? k0 + 2 : k0 + R;
          if (k0 < NOP) epi(q - 1, k0);
          if (k0 + 1 < k1 && k0 + 1 < NOP) epi(q - 1, k0 + 1);
          if (k0 + 2 < k1 && k0 + 2 < NOP) epi(q - 1, k0 + 2);
        }
        if (g == 15) settle_h(4, 9);
        if (g == 35) settle_h(9, 14);
        if (q < 3 && t == 3) { Bh[o] = __builtin_amdgcn_raw_buffer_load_b128(rh, lane * 16, ko[s], SC1); GAP(); }
        if (g == 41) { res = __builtin_amdgcn_raw_buffer_load_b64(rws, rvo, WS_X1 + layer * ACT_BYTES + q * xq, SC1); }
        GAP();
      });
      STAMP(24 + 24 * layer + q);
    });
    asm volatile("s_nop 7" : "+v"(aa[4]), "+v"(aa[5]), "+v"(aa[6]), "+v"(aa[7]));
    sfor<0, NOP>([&](auto kc) AI { epi(3, decltype(kc)::value); });
    if (layer == 0) asm volatile("s_barrier" ::: "memory");
    STAMP(28 + 24 * layer);
  };

  conv(std::integral_constant<int, 0>{});
  ffn(std::integral_constant<int, 0>{});
  conv(std::integral_constant<int, 1>{});
  ffn(std::integral_constant<int, 1>{});
  STAMP_OUT();
  } else {
    VMWAIT(0); BAR();
    if (lane == 0 || lane == 16) st_gran(p.ws, st1off, st1, tagb + T_ST);
    helper_main(p, lds, tagb, xg, idx - 28, lane, wv);
    asm volatile(".Lfused_code_end:");
    }
}

}

extern "C" void kernel_launch(void* const* d_in, const int* in_sizes, int n_in, void* d_out, int out_size, void* d_ws, size_t ws_size,
                              hipStream_t stream) {
  Params p;
  p.u = (const u16*)d_in[0]; p.lar = (const u16*)d_in[1]; p.aim = (const u16*)d_in[2]; p.cre = (const u16*)d_in[3];
  p.cim = (const u16*)d_in[4]; p.ldt = (const u16*)d_in[5]; p.dsk = (const u16*)d_in[6];
  p.wg = (const u16*)d_in[8]; p.wu = (const u16*)d_in[9]; p.wd = (const u16*)d_in[10];
  p.out = (u16*)d_out; p.ws = (unsigned char*)d_ws;
  (void)hipFuncSetAttribute((const void*)fused_kernel, hipFuncAttributeMaxDynamicSharedMemorySize, LDS_BYTES);
  hipLaunchKernelGGL(fused_kernel, dim3(256), dim3(256), LDS_BYTES, stream, p);
}
```
